```python
import torch

def _softmax(x, axis=-1):
    return torch.softmax(x, dim=axis)
def _gelu(x):
    return torch.nn.functional.gelu(x, approximate="tanh")
def _silu(x):
    return torch.nn.functional.silu(x)
def _sigmoid(x):
    return torch.sigmoid(x)
def _layernorm(x, axis=-1, eps=1e-5):
    m = x.mean(dim=axis, keepdim=True)
    v = x.var(dim=axis, keepdim=True, unbiased=False)
    return (x - m) / torch.sqrt(v + eps)
def _scatter_add(tgt, idx, src, axis=0):
    return tgt.clone().index_add_(axis, idx, src)
def _segment_sum(src, seg, n):
    out = torch.zeros((n,) + src.shape[1:], dtype=src.dtype)
    return out.index_add_(0, seg, src)
def _pad(x, pw):
    flat = tuple(v for pair in reversed(pw) for v in pair)
    return torch.nn.functional.pad(x, flat)

kernel_name = 'interleave_layers__linear_kernel_hedgehog__softmax_full_mha__prompt'

def setup_inputs():

    _w = (lambda a: (lambda t: t.to(torch.bfloat16) if t.is_floating_point() else t)(torch.as_tensor(a)))
    def _A_setup():
        B, S, D, H = 1, 2048, 576, 32
        Dh = D // H
        return {
            "x": _w((lambda *s: torch.randn(*s, dtype=torch.bfloat16) * 0.05)(B, S, D)),
            "wqkv": _w((lambda *s: torch.randn(*s, dtype=torch.bfloat16) * 0.4)(D, 3 * D)),
            "w_phi": _w((lambda *s: torch.randn(*s, dtype=torch.bfloat16) * 0.4)(H, Dh, Dh) * (Dh ** -0.5)),
            "wo": _w((lambda *s: torch.randn(*s, dtype=torch.bfloat16) * 0.05)(D, D)),
        }
    _A_in = _A_setup()

    def _B_setup():
        B, S, D = 1, 2048, 576
        return {
            "x": _w((lambda *s: torch.randn(*s, dtype=torch.bfloat16) * 0.05)(B, S, D)),
            "wqkv": _w((lambda *s: torch.randn(*s, dtype=torch.bfloat16) * 0.1)(D, 3 * D)),
            "wo": _w((lambda *s: torch.randn(*s, dtype=torch.bfloat16) * 0.05)(D, D)),
        }
    _B_in = _B_setup()

    return {
        "A_x": _A_in["x"],
        "A_wqkv": _A_in["wqkv"],
        "A_w_phi": _A_in["w_phi"],
        "A_wo": _A_in["wo"],
        "B_x": _B_in["x"],
        "B_wqkv": _B_in["wqkv"],
        "B_wo": _B_in["wo"],
    }

def reference(A_x, A_wqkv, A_w_phi, A_wo, B_x, B_wqkv, B_wo):
    def _A_ref(x, wqkv, w_phi, wo):
        B, S, D = x.shape
        H = 32; Dh = D // H
        qkv = (lambda x, *d: x.permute(*d))((x @ wqkv).reshape(B, S, 3, H, Dh), 2, 0, 3, 1, 4)
        q, k, v = qkv[0], qkv[1], qkv[2]
        pq = torch.einsum("bhsd,hdf->bhsf", q, w_phi)
        pk = torch.einsum("bhsd,hdf->bhsf", k, w_phi)
        phi_q = torch.concatenate([_softmax(pq, axis=-1), _softmax(-pq, axis=-1)], axis=-1)
        phi_k = torch.concatenate([_softmax(pk, axis=-1), _softmax(-pk, axis=-1)], axis=-1)
        kv = torch.einsum("bhsf,bhsd->bhfd", phi_k, v)
        z  = torch.einsum("bhsf->bhf", phi_k)
        num = torch.einsum("bhqf,bhfd->bhqd", phi_q, kv)
        den = torch.einsum("bhqf,bhf->bhq", phi_q, z)[..., None] + 1e-6
        o = num / den
        return (lambda x, *d: x.permute(*d))(o, 0, 2, 1, 3).reshape(B, S, D) @ wo

    def _B_ref(x, wqkv, wo):
        B, S, D = x.shape
        H = 32; Dh = D // H
        qkv = (lambda x, *d: x.permute(*d))((x @ wqkv).reshape(B, S, 3, H, Dh), 2, 0, 3, 1, 4)
        q, k, v = qkv[0], qkv[1], qkv[2]
        scale = Dh ** -0.5
        scores = torch.einsum("bhqd,bhkd->bhqk", q, k) * scale
        p = _softmax(scores, axis=-1)
        o = torch.einsum("bhqk,bhkd->bhqd", p, v)
        return (lambda x, *d: x.permute(*d))(o, 0, 2, 1, 3).reshape(B, S, D) @ wo

    x = A_x
    x = x + _A_ref(_layernorm(x, axis=-1), A_wqkv, A_w_phi, A_wo)
    x = x + _B_ref(_layernorm(x, axis=-1), B_wqkv, B_wo)
    x = x + _A_ref(_layernorm(x, axis=-1), A_wqkv, A_w_phi, A_wo)
    x = x + _B_ref(_layernorm(x, axis=-1), B_wqkv, B_wo)
    return x
```

```cpp
#include <hip/hip_runtime.h>
#include <hip/hip_bf16.h>
#include <stdint.h>

typedef __attribute__((ext_vector_type(8))) short bf16x8;
typedef __attribute__((ext_vector_type(4))) float f32x4;
typedef __attribute__((ext_vector_type(16))) float f32x16;
typedef __attribute__((ext_vector_type(4))) unsigned u32x4;
typedef unsigned short u16;

constexpr int S_ = 2048, D_ = 576, H_ = 32, DH = 18, N3 = 1728;

__device__ __forceinline__ float bf2f(u16 v) { return __uint_as_float(((unsigned)v) << 16); }
__device__ __forceinline__ u16 f2bf(float f) {
  __hip_bfloat16 b = __float2bfloat16(f);
  return *reinterpret_cast<u16*>(&b);
}
__device__ __forceinline__ float rbf(float f) { return bf2f(f2bf(f)); }
__device__ __forceinline__ unsigned pack2(float lo, float hi) {
  return (unsigned)f2bf(lo) | ((unsigned)f2bf(hi) << 16);
}

__global__ __launch_bounds__(256) void pack_w_kernel(const u16* __restrict__ Wa, const u16* __restrict__ Wb,
                                                     const u16* __restrict__ Wc, const u16* __restrict__ Wd,
                                                     u16* __restrict__ Pa, u16* __restrict__ Pb,
                                                     u16* __restrict__ Pc, u16* __restrict__ Pd) {
  const int which = blockIdx.y;
  const u16* W = which == 0 ? Wa : which == 1 ? Wb : which == 2 ? Wc : Wd;
  u16* P = which == 0 ? Pa : which == 1 ? Pb : which == 2 ? Pc : Pd;
  const int N = which < 2 ? N3 : D_;
  const int idx = blockIdx.x * 256 + threadIdx.x;
  const int lane = idx & 63, t = idx >> 6;
  const int kb = t % 18, nb = t / 18;
  if (nb >= N / 16) return;
  const int k0 = kb * 32 + 8 * (lane >> 4), n = nb * 16 + (lane & 15);
  u16 v[8];
#pragma unroll
  for (int j = 0; j < 8; ++j) v[j] = W[(size_t)(k0 + j) * N + n];
  u32x4 o;
#pragma unroll
  for (int j = 0; j < 4; ++j) o[j] = (unsigned)v[2 * j] | ((unsigned)v[2 * j + 1] << 16);
  *reinterpret_cast<u32x4*>(P + ((size_t)(nb * 18 + kb) * 64 + lane) * 8) = o;
}

template <int NCB, bool LN, bool RES>
__global__ __launch_bounds__(256) void gemm_kernel(const u16* __restrict__ A, const u16* __restrict__ Wp,
                                                   const u16* __restrict__ R, u16* __restrict__ C, int ldc) {
  __shared__ __attribute__((aligned(16))) u16 As[4 * 18 * 64 * 8];
  const int tid = threadIdx.x, lane = tid & 63, wid = tid >> 6;
  const int row0 = blockIdx.y * 64;
  const int nb0 = blockIdx.x * (4 * NCB) + wid * NCB;
  {
    const int r = tid >> 2, p = tid & 3;
    const u16* src = A + (size_t)(row0 + r) * D_ + p * 144;
    bf16x8 v[18];
#pragma unroll
    for (int c = 0; c < 18; ++c) v[c] = *reinterpret_cast<const bf16x8*>(src + c * 8);
    if (LN) {
      float s = 0.f;
#pragma unroll
      for (int c = 0; c < 18; ++c)
#pragma unroll
        for (int j = 0; j < 8; ++j) s += bf2f((u16)v[c][j]);
      s += __shfl_xor(s, 1);
      s += __shfl_xor(s, 2);
      const float mean = s * (1.0f / D_);
      float q = 0.f;
#pragma unroll
      for (int c = 0; c < 18; ++c)
#pragma unroll
        for (int j = 0; j < 8; ++j) {
          const float d = bf2f((u16)v[c][j]) - mean;
          q += d * d;
        }
      q += __shfl_xor(q, 1);
      q += __shfl_xor(q, 2);
      const float mb = rbf(mean);
      const float var = rbf(q * (1.0f / D_));
      const float sd = rbf(sqrtf(rbf(var + 1e-5f)));
#pragma unroll
      for (int c = 0; c < 18; ++c)
#pragma unroll
        for (int j = 0; j < 8; ++j) {
          const float d = rbf(bf2f((u16)v[c][j]) - mb);
          v[c][j] = (short)f2bf(d / sd);
        }
    }
#pragma unroll
    for (int c = 0; c < 18; ++c) {
      const int k = p * 144 + c * 8;
      const int ks = k >> 5, kg = (k >> 3) & 3;
      *reinterpret_cast<bf16x8*>(As + ((((r >> 4) * 18 + ks) * 64) + kg * 16 + (r & 15)) * 8) = v[c];
    }
  }
  __syncthreads();
  f32x4 acc[4][NCB];
#pragma unroll
  for (int i = 0; i < 4; ++i)
#pragma unroll
    for (int j = 0; j < NCB; ++j) acc[i][j] = f32x4{0.f, 0.f, 0.f, 0.f};
  const bf16x8* Bp = reinterpret_cast<const bf16x8*>(Wp);
#pragma unroll 2
  for (int ks = 0; ks < 18; ++ks) {
    bf16x8 b[NCB];
#pragma unroll
    for (int j = 0; j < NCB; ++j) b[j] = Bp[((size_t)(nb0 + j) * 18 + ks) * 64 + lane];
#pragma unroll
    for (int i = 0; i < 4; ++i) {
      const bf16x8 a = *reinterpret_cast<const bf16x8*>(As + ((i * 18 + ks) * 64 + lane) * 8);
#pragma unroll
      for (int j = 0; j < NCB; ++j) acc[i][j] = __builtin_amdgcn_mfma_f32_16x16x32_bf16(a, b[j], acc[i][j], 0, 0, 0);
    }
  }
#pragma unroll
  for (int i = 0; i < 4; ++i)
#pragma unroll
    for (int j = 0; j < NCB; ++j)
#pragma unroll
      for (int r = 0; r < 4; ++r) {
        const int row = row0 + i * 16 + (lane >> 4) * 4 + r;
        const int col = (nb0 + j) * 16 + (lane & 15);
        float v = rbf(acc[i][j][r]);
        if (RES) v += bf2f(R[(size_t)row * D_ + col]);
        C[(size_t)row * ldc + col] = f2bf(v);
      }
}

__device__ __forceinline__ void hedgehog(const float* in, const float* __restrict__ wphi, float* out) {
  float p[DH];
#pragma unroll
  for (int f = 0; f < DH; ++f) p[f] = 0.f;
#pragma unroll
  for (int d = 0; d < DH; ++d)
#pragma unroll
    for (int f = 0; f < DH; ++f) p[f] = fmaf(in[d], wphi[d * DH + f], p[f]);
  float mx = -INFINITY, mn = INFINITY;
#pragma unroll
  for (int f = 0; f < DH; ++f) {
    p[f] = rbf(p[f]);
    mx = fmaxf(mx, p[f]);
    mn = fminf(mn, p[f]);
  }
  float s1 = 0.f, s2 = 0.f;
#pragma unroll
  for (int f = 0; f < DH; ++f) {
    out[f] = __expf(p[f] - mx);
    out[DH + f] = __expf(mn - p[f]);
    s1 += out[f];
    s2 += out[DH + f];
  }
  const float r1 = 1.f / s1, r2 = 1.f / s2;
#pragma unroll
  for (int f = 0; f < DH; ++f) {
    out[f] = rbf(out[f] * r1);
    out[DH + f] = rbf(out[DH + f] * r2);
  }
}

__global__ __launch_bounds__(256) void kv_kernel(const u16* __restrict__ qkv, const u16* __restrict__ wphi_g,
                                                 float* __restrict__ part) {
  __shared__ float wphi[DH * DH];
  __shared__ float phi[256][37];
  __shared__ float vv[256][19];
  const int h = blockIdx.y, ch = blockIdx.x, tid = threadIdx.x;
  for (int i = tid; i < DH * DH; i += 256) wphi[i] = bf2f(wphi_g[h * DH * DH + i]);
  __syncthreads();
  const int t = ch * 256 + tid;
  float kk[DH], ph[36];
#pragma unroll
  for (int d = 0; d < DH; ++d) {
    kk[d] = bf2f(qkv[(size_t)t * N3 + D_ + h * DH + d]);
    vv[tid][d] = bf2f(qkv[(size_t)t * N3 + 2 * D_ + h * DH + d]);
  }
  vv[tid][18] = 1.f;
  hedgehog(kk, wphi, ph);
#pragma unroll
  for (int f = 0; f < 36; ++f) phi[tid][f] = ph[f];
  __syncthreads();
  for (int o = tid; o < 36 * 19; o += 256) {
    const int f = o / 19, d = o % 19;
    float s = 0.f;
    for (int i = 0; i < 256; ++i) s = fmaf(phi[i][f], vv[i][d], s);
    part[((size_t)h * 8 + ch) * 684 + o] = s;
  }
}

__global__ __launch_bounds__(256) void lin_o_kernel(const u16* __restrict__ qkv, const u16* __restrict__ wphi_g,
                                                    const float* __restrict__ part, u16* __restrict__ o) {
  __shared__ float wphi[DH * DH];
  __shared__ float kv[36 * 19];
  const int h = blockIdx.y, ch = blockIdx.x, tid = threadIdx.x;
  for (int i = tid; i < DH * DH; i += 256) wphi[i] = bf2f(wphi_g[h * DH * DH + i]);
  for (int i = tid; i < 684; i += 256) {
    float s = 0.f;
#pragma unroll
    for (int c = 0; c < 8; ++c) s += part[((size_t)h * 8 + c) * 684 + i];
    kv[i] = rbf(s);
  }
  __syncthreads();
  const int t = ch * 256 + tid;
  float qq[DH], ph[36];
#pragma unroll
  for (int d = 0; d < DH; ++d) qq[d] = bf2f(qkv[(size_t)t * N3 + h * DH + d]);
  hedgehog(qq, wphi, ph);
  float den = 0.f;
#pragma unroll
  for (int f = 0; f < 36; ++f) den = fmaf(ph[f], kv[f * 19 + 18], den);
  den = rbf(rbf(den) + 1e-6f);
#pragma unroll
  for (int d = 0; d < DH; ++d) {
    float n = 0.f;
#pragma unroll
    for (int f = 0; f < 36; ++f) n = fmaf(ph[f], kv[f * 19 + d], n);
    o[(size_t)t * D_ + h * DH + d] = f2bf(rbf(n) / den);
  }
}

constexpr int KT = 128;
constexpr int VSTR = KT + 8;
__global__ __launch_bounds__(256) void attn_kernel(const u16* __restrict__ qkv, u16* __restrict__ o) {
  __shared__ __attribute__((aligned(16))) u16 Ks[KT * 32];
  __shared__ __attribute__((aligned(16))) u16 Vt[32 * VSTR];
  const int h = blockIdx.y, tid = threadIdx.x, lane = tid & 63, wid = tid >> 6;
  const int q0 = blockIdx.x * 128 + wid * 32;
  const int l31 = lane & 31, hh = lane >> 5;
  for (int i = tid; i < 14 * VSTR; i += 256) Vt[18 * VSTR + i] = (i < VSTR) ? (u16)0x3f80 : (u16)0;
  const float qs = 0.23570226039551584f * 1.4426950408889634f;
  bf16x8 qf[2];
  {
    const u16* qp = qkv + (size_t)(q0 + l31) * N3 + h * DH;
#pragma unroll
    for (int s = 0; s < 2; ++s)
#pragma unroll
      for (int j = 0; j < 8; ++j) {
        const int d = s * 16 + hh * 8 + j;
        qf[s][j] = d < DH ? (short)f2bf(bf2f(qp[d < DH ? d : 0]) * qs) : (short)0;
      }
  }
  f32x16 oacc, cm;
#pragma unroll
  for (int r = 0; r < 16; ++r) { oacc[r] = 0.f; cm[r] = 0.f; }
  bool first = true;
  for (int kt = 0; kt < S_ / KT; ++kt) {
    __syncthreads();
    if (tid < KT) {
      const unsigned* kp = reinterpret_cast<const unsigned*>(qkv + (size_t)(kt * KT + tid) * N3 + D_ + h * DH);
      unsigned w[16];
#pragma unroll
      for (int i = 0; i < 9; ++i) w[i] = kp[i];
#pragma unroll
      for (int i = 9; i < 16; ++i) w[i] = 0;
#pragma unroll
      for (int c = 0; c < 4; ++c) {
        u32x4 x = {w[4 * c], w[4 * c + 1], w[4 * c + 2], w[4 * c + 3]};
        *reinterpret_cast<u32x4*>(Ks + tid * 32 + ((c ^ ((tid >> 2) & 3)) * 8)) = x;
      }
    } else {
      const int key = tid - KT;
      const u16* vp = qkv + (size_t)(kt * KT + key) * N3 + 2 * D_ + h * DH;
      const int k16 = key & 15;
      const int pos = (key & ~15) + ((k16 >> 2) & 1) * 8 + (k16 & 3) + 4 * (k16 >> 3);
#pragma unroll
      for (int d = 0; d < DH; ++d) Vt[d * VSTR + pos] = vp[d];
    }
    __syncthreads();
#pragma unroll
    for (int t = 0; t < KT / 32; ++t) {
      const int key = t * 32 + l31;
      const bf16x8 k0 = *reinterpret_cast<const bf16x8*>(Ks + key * 32 + (((0 + hh) ^ ((key >> 2) & 3)) * 8));
      const bf16x8 k1 = *reinterpret_cast<const bf16x8*>(Ks + key * 32 + (((2 + hh) ^ ((key >> 2) & 3)) * 8));
      f32x16 s = __builtin_amdgcn_mfma_f32_32x32x16_bf16(k0, qf[0], cm, 0, 0, 0);
      s = __builtin_amdgcn_mfma_f32_32x32x16_bf16(k1, qf[1], s, 0, 0, 0);
      float tmax = s[0];
#pragma unroll
      for (int r = 1; r < 16; ++r) tmax = fmaxf(tmax, s[r]);
      tmax = fmaxf(tmax, __shfl_xor(tmax, 32));
      if (first || __any(tmax > 0.f)) {
        const float delta = first ? tmax : fmaxf(tmax, 0.f);
        const float sc = first ? 0.f : __builtin_amdgcn_exp2f(-delta);
#pragma unroll
        for (int r = 0; r < 16; ++r) {
          s[r] -= delta;
          cm[r] -= delta;
          oacc[r] *= sc;
        }
        first = false;
      }
      bf16x8 pf[2];
#pragma unroll
      for (int ss = 0; ss < 2; ++ss) {
        u32x4 w;
#pragma unroll
        for (int j = 0; j < 4; ++j)
          w[j] = pack2(__builtin_amdgcn_exp2f(s[8 * ss + 2 * j]), __builtin_amdgcn_exp2f(s[8 * ss + 2 * j + 1]));
        pf[ss] = __builtin_bit_cast(bf16x8, w);
      }
#pragma unroll
      for (int ss = 0; ss < 2; ++ss) {
        const bf16x8 vf = *reinterpret_cast<const bf16x8*>(Vt + l31 * VSTR + t * 32 + ss * 16 + hh * 8);
        oacc = __builtin_amdgcn_mfma_f32_32x32x16_bf16(vf, pf[ss], oacc, 0, 0, 0);
      }
    }
  }
  const float l = __shfl(oacc[10], l31);
  const float rl = 1.f / l;
  u16* op = o + (size_t)(q0 + l31) * D_ + h * DH;
#pragma unroll
  for (int r = 0; r < 16; ++r) {
    const int d = (r & 3) + 8 * (r >> 2) + 4 * hh;
    if (d < DH) op[d] = f2bf(oacc[r] * rl);
  }
}

extern "C" void kernel_launch(void* const* d_in, const int* in_sizes, int n_in, void* d_out, int out_size,
                              void* d_ws, size_t ws_size, hipStream_t stream) {
  const u16* A_x = (const u16*)d_in[0];
  const u16* A_wqkv = (const u16*)d_in[1];
  const u16* A_wphi = (const u16*)d_in[2];
  const u16* A_wo = (const u16*)d_in[3];
  const u16* B_wqkv = (const u16*)d_in[5];
  const u16* B_wo = (const u16*)d_in[6];
  char* ws = (char*)d_ws;
  size_t off = 0;
  auto alloc = [&](size_t bytes) { void* p = ws + off; off += (bytes + 255) & ~(size_t)255; return p; };
  u16* PAq = (u16*)alloc((size_t)D_ * N3 * 2);
  u16* PBq = (u16*)alloc((size_t)D_ * N3 * 2);
  u16* PAo = (u16*)alloc((size_t)D_ * D_ * 2);
  u16* PBo = (u16*)alloc((size_t)D_ * D_ * 2);
  u16* qkv = (u16*)alloc((size_t)S_ * N3 * 2);
  u16* ob = (u16*)alloc((size_t)S_ * D_ * 2);
  u16* x1 = (u16*)alloc((size_t)S_ * D_ * 2);
  u16* x2 = (u16*)alloc((size_t)S_ * D_ * 2);
  u16* x3 = (u16*)alloc((size_t)S_ * D_ * 2);
  float* part = (float*)alloc((size_t)H_ * 8 * 684 * 4);

  pack_w_kernel<<<dim3(108 * 18 * 64 / 256, 4), 256, 0, stream>>>(A_wqkv, B_wqkv, A_wo, B_wo, PAq, PBq, PAo, PBo);

  const u16* xin = A_x;
  u16* xs[4] = {x1, x2, x3, (u16*)d_out};
  for (int layer = 0; layer < 4; ++layer) {
    const bool isA = (layer & 1) == 0;
    gemm_kernel<3, true, false><<<dim3(9, 32), 256, 0, stream>>>(xin, isA ? PAq : PBq, nullptr, qkv, N3);
    if (isA) {
      kv_kernel<<<dim3(8, 32), 256, 0, stream>>>(qkv, A_wphi, part);
      lin_o_kernel<<<dim3(8, 32), 256, 0, stream>>>(qkv, A_wphi, part, ob);
    } else {
      attn_kernel<<<dim3(16, 32), 256, 0, stream>>>(qkv, ob);
    }
    gemm_kernel<1, false, true><<<dim3(9, 32), 256, 0, stream>>>(ob, isA ? PAo : PBo, xin, xs[layer], D_);
    xin = xs[layer];
  }
}
```

```cpp
#include <hip/hip_runtime.h>
#include <hip/hip_bf16.h>
#include <stdint.h>
#include <type_traits>

typedef __attribute__((ext_vector_type(8))) short bf16x8;
typedef __attribute__((ext_vector_type(4))) float f32x4;
typedef __attribute__((ext_vector_type(16))) float f32x16;
typedef __attribute__((ext_vector_type(4))) unsigned u32x4;
typedef __attribute__((ext_vector_type(2))) unsigned u32x2;
typedef unsigned short u16;

constexpr int S_ = 2048, D_ = 576, H_ = 32, DH = 18, N3 = 1728;

__device__ __forceinline__ float bf2f(u16 v) { return __uint_as_float(((unsigned)v) << 16); }
__device__ __forceinline__ u16 f2bf(float f) {
  __hip_bfloat16 b = __float2bfloat16(f);
  return *reinterpret_cast<u16*>(&b);
}
__device__ __forceinline__ float rbf(float f) { return bf2f(f2bf(f)); }
typedef __attribute__((ext_vector_type(2))) __bf16 bf16x2_t;
typedef __attribute__((ext_vector_type(2))) float f32x2;
__device__ __forceinline__ unsigned pack2(float lo, float hi) {
  f32x2 f = {lo, hi};
  return __builtin_bit_cast(unsigned, __builtin_convertvector(f, bf16x2_t));
}
__device__ __forceinline__ float lo16(unsigned v) { return __uint_as_float(v << 16); }
__device__ __forceinline__ float hi16(unsigned v) { return __uint_as_float(v & 0xffff0000u); }

__global__ __launch_bounds__(256) void pack_w_kernel(const u16* __restrict__ Wa, const u16* __restrict__ Wb,
                                                     const u16* __restrict__ Wc, const u16* __restrict__ Wd,
                                                     u16* __restrict__ Pa, u16* __restrict__ Pb,
                                                     u16* __restrict__ Pc, u16* __restrict__ Pd,
                                                     float* __restrict__ csum) {
  const int which = blockIdx.y;
  if (which >= 4) {
    __shared__ float red[4][64];
    if (blockIdx.x >= 27) return;
    const u16* W = which == 4 ? Wa : Wb;
    const int c = threadIdx.x & 63, g = threadIdx.x >> 6;
    const int n = blockIdx.x * 64 + c;
    float sum = 0.f;
#pragma unroll 16
    for (int k = 0; k < 144; ++k) sum += bf2f(W[(size_t)(g * 144 + k) * N3 + n]);
    red[g][c] = sum;
    __syncthreads();
    if (g == 0) csum[(which - 4) * N3 + n] = (red[0][c] + red[1][c]) + (red[2][c] + red[3][c]);
    return;
  }
  const u16* W = which == 0 ? Wa : which == 1 ? Wb : which == 2 ? Wc : Wd;
  u16* P = which == 0 ? Pa : which == 1 ? Pb : which == 2 ? Pc : Pd;
  const int N = which < 2 ? N3 : D_;
  const int idx = blockIdx.x * 256 + threadIdx.x;
  const int lane = idx & 63, t = idx >> 6;
  const int kb = t % 18, nb = t / 18;
  if (nb >= N / 16) return;
  const int k0 = kb * 32 + 8 * (lane >> 4), n = nb * 16 + (lane & 15);
  u16 v[8];
#pragma unroll
  for (int j = 0; j < 8; ++j) v[j] = W[(size_t)(k0 + j) * N + n];
  u32x4 o;
#pragma unroll
  for (int j = 0; j < 4; ++j) o[j] = (unsigned)v[2 * j] | ((unsigned)v[2 * j + 1] << 16);
  *reinterpret_cast<u32x4*>(P + ((size_t)(nb * 18 + kb) * 64 + lane) * 8) = o;
}

template <int NCB, int MODE>
__global__ __launch_bounds__(512, 2) void gemm_kernel(const u16* __restrict__ A, const u16* __restrict__ Wp,
                                                      const u16* __restrict__ R, const float* __restrict__ csum,
                                                      u16* __restrict__ C) {
  __shared__ __attribute__((aligned(16))) u16 As[64 * 72 * 8];
  __shared__ float stats[2][64][2];
  constexpr int PF = 4;
  constexpr bool LN = MODE != 2;
  const int tid = threadIdx.x, lane = tid & 63, wid = tid >> 6;
  const int cg = wid & 3, kh = wid >> 2;
  const int row0 = blockIdx.y * 64;
  const int nb0 = blockIdx.x * (4 * NCB) + cg * NCB;
  const bf16x8* Bp = reinterpret_cast<const bf16x8*>(Wp) + ((size_t)nb0 * 18 + kh * 9) * 64 + lane;
  bf16x8 bq[PF][NCB];
#pragma unroll
  for (int s = 0; s < PF; ++s)
#pragma unroll
    for (int j = 0; j < NCB; ++j) bq[s][j] = Bp[(j * 18 + s) * 64];
  {
    const u32x4* src = reinterpret_cast<const u32x4*>(A + (size_t)(row0 + wid * 8) * D_);
    u32x4 v[9];
#pragma unroll
    for (int i = 0; i < 9; ++i) v[i] = src[i * 64 + lane];
#pragma unroll
    for (int i = 0; i < 9; ++i) {
      const int idx = i * 64 + lane;
      const int r = idx / 72 + wid * 8, ch = idx - (idx / 72) * 72;
      *reinterpret_cast<u32x4*>(As + (r * 72 + (ch ^ ((r >> 1) & 7))) * 8) = v[i];
    }
  }
  __syncthreads();
  const int aoff = (lane & 15) * 72 * 8, asw = ((lane & 15) >> 1) & 7;
  const bool vblk = (MODE == 1) && (nb0 * 16 >= 2 * D_);
  f32x4 acc[4][NCB];
#pragma unroll
  for (int i = 0; i < 4; ++i)
#pragma unroll
    for (int j = 0; j < NCB; ++j) acc[i][j] = f32x4{0.f, 0.f, 0.f, 0.f};
  float ssum = 0.f, ssq = 0.f, shift = 0.f;
  if (LN) shift = bf2f(As[(cg * 16 + (lane & 15)) * 72 * 8 + (asw * 8)]);
  auto kloop = [&](auto swapped) {
#pragma unroll
    for (int kk = 0; kk < 9; ++kk) {
      const int ks = kh * 9 + kk;
      bf16x8 b[NCB];
#pragma unroll
      for (int j = 0; j < NCB; ++j) b[j] = bq[kk % PF][j];
      if (kk + PF < 9) {
#pragma unroll
        for (int j = 0; j < NCB; ++j) bq[kk % PF][j] = Bp[(j * 18 + kk + PF) * 64];
      }
#pragma unroll
      for (int i = 0; i < 4; ++i) {
        const bf16x8 a =
            *reinterpret_cast<const bf16x8*>(As + aoff + (i * 16 * 72 + ((ks * 4 + (lane >> 4)) ^ asw)) * 8);
        if (LN && i == cg) {
          const u32x4 w = __builtin_bit_cast(u32x4, a);
#pragma unroll
          for (int e = 0; e < 4; ++e) {
            const float d0 = lo16(w[e]) - shift, d1 = hi16(w[e]) - shift;
            ssum += d0 + d1;
            ssq = fmaf(d0, d0, ssq);
            ssq = fmaf(d1, d1, ssq);
          }
        }
#pragma unroll
        for (int j = 0; j < NCB; ++j) {
          if (decltype(swapped)::value)
            acc[i][j] = __builtin_amdgcn_mfma_f32_16x16x32_bf16(b[j], a, acc[i][j], 0, 0, 0);
          else
            acc[i][j] = __builtin_amdgcn_mfma_f32_16x16x32_bf16(a, b[j], acc[i][j], 0, 0, 0);
        }
      }
    }
  };
  if (!vblk) kloop(std::integral_constant<bool, true>{});
  else kloop(std::integral_constant<bool, false>{});
  if (LN) {
    ssum += __shfl_xor(ssum, 16);
    ssq += __shfl_xor(ssq, 16);
    ssum += __shfl_xor(ssum, 32);
    ssq += __shfl_xor(ssq, 32);
    if (lane < 16) {
      stats[kh][cg * 16 + lane][0] = ssum;
      stats[kh][cg * 16 + lane][1] = ssq;
    }
  }
  __syncthreads();
  float* red = reinterpret_cast<float*>(As);
  if (kh == 1) {
#pragma unroll
    for (int i = 0; i < 4; ++i)
#pragma unroll
      for (int j = 0; j < NCB; ++j)
        *reinterpret_cast<f32x4*>(red + (((cg * 4 + i) * NCB + j) * 64 + lane) * 4) = acc[i][j];
  } else if (LN) {
    if (cg == 0) {
      const float s1 = stats[0][lane][0] + stats[1][lane][0];
      const float s2 = stats[0][lane][1] + stats[1][lane][1];
      const float dm = s1 * (1.0f / D_);
      const float sh = bf2f(A[(size_t)(row0 + lane) * D_]);
      const float mean = sh + dm;
      const float var = rbf(fmaxf(s2 * (1.0f / D_) - dm * dm, 0.f));
      const float sd = rbf(sqrtf(rbf(var + 1e-5f)));
      stats[0][lane][0] = rbf(mean);
      stats[0][lane][1] = 1.0f / sd;
    }
  }
  __syncthreads();
  if (kh == 1) return;
#pragma unroll
  for (int i = 0; i < 4; ++i)
#pragma unroll
    for (int j = 0; j < NCB; ++j) {
      const f32x4 o = *reinterpret_cast<const f32x4*>(red + (((cg * 4 + i) * NCB + j) * 64 + lane) * 4);
      f32x4 v = acc[i][j] + o;
      if (MODE == 2) {
        const int t = row0 + i * 16 + (lane & 15);
        const int n = (nb0 + j) * 16 + (lane >> 4) * 4;
        const u32x2 rr = *reinterpret_cast<const u32x2*>(R + (size_t)t * D_ + n);
        const unsigned p0 = pack2(v[0], v[1]), p1 = pack2(v[2], v[3]);
        u32x2 w = {pack2(lo16(p0) + lo16(rr[0]), hi16(p0) + hi16(rr[0])),
                   pack2(lo16(p1) + lo16(rr[1]), hi16(p1) + hi16(rr[1]))};
        *reinterpret_cast<u32x2*>(C + (size_t)t * D_ + n) = w;
      } else if (!vblk) {
        const int tl = i * 16 + (lane & 15);
        const int t = row0 + tl;
        const int n = (nb0 + j) * 16 + (lane >> 4) * 4;
        const float mb = stats[0][tl][0], rs = stats[0][tl][1];
        const f32x4 cs = *reinterpret_cast<const f32x4*>(csum + n);
#pragma unroll
        for (int e = 0; e < 4; ++e) v[e] = (v[e] - mb * cs[e]) * rs;
        if (MODE == 0) {
          u32x2 w = {pack2(v[0], v[1]), pack2(v[2], v[3])};
          *reinterpret_cast<u32x2*>(C + (size_t)t * N3 + n) = w;
        } else {
          const int which = n >= D_;
          const int c0 = n - which * D_;
          const int h0 = c0 / DH, d0 = c0 - h0 * DH;
          const int c1 = c0 + 2;
          const int h1 = c1 / DH, d1 = c1 - h1 * DH;
          u16* base = C + (size_t)which * (H_ * S_ * 32);
          *reinterpret_cast<unsigned*>(base + ((size_t)h0 * S_ + t) * 32 + d0) = pack2(v[0], v[1]);
          *reinterpret_cast<unsigned*>(base + ((size_t)h1 * S_ + t) * 32 + d1) = pack2(v[2], v[3]);
        }
      } else {
        const int n = (nb0 + j) * 16 + (lane & 15);
        const int c = n - 2 * D_;
        const int h = c / DH, d = c - h * DH;
        const int g = lane >> 4;
        const float cs = csum[n];
#pragma unroll
        for (int e = 0; e < 4; ++e) {
          const int tl = i * 16 + g * 4 + e;
          v[e] = (v[e] - stats[0][tl][0] * cs) * stats[0][tl][1];
        }
        const int pos = row0 + i * 16 + (g & 1) * 8 + (g >> 1) * 4;
        u16* base = C + (size_t)2 * (H_ * S_ * 32);
        u32x2 w = {pack2(v[0], v[1]), pack2(v[2], v[3])};
        *reinterpret_cast<u32x2*>(base + ((size_t)h * 32 + d) * S_ + pos) = w;
      }
    }
}

__device__ __forceinline__ void hedgehog(const float* in, const float* __restrict__ wphi, float* out) {
  float p[DH];
#pragma unroll
  for (int f = 0; f < DH; ++f) p[f] = 0.f;
#pragma unroll
  for (int d = 0; d < DH; ++d)
#pragma unroll
    for (int f = 0; f < DH; ++f) p[f] = fmaf(in[d], wphi[d * DH + f], p[f]);
  float mx = -INFINITY, mn = INFINITY;
#pragma unroll
  for (int f = 0; f < DH; ++f) {
    p[f] = rbf(p[f]);
    mx = fmaxf(mx, p[f]);
    mn = fminf(mn, p[f]);
  }
  float s1 = 0.f, s2 = 0.f;
#pragma unroll
  for (int f = 0; f < DH; ++f) {
    out[f] = __expf(p[f] - mx);
    out[DH + f] = __expf(mn - p[f]);
    s1 += out[f];
    s2 += out[DH + f];
  }
  const float r1 = 1.f / s1, r2 = 1.f / s2;
#pragma unroll
  for (int f = 0; f < DH; ++f) {
    out[f] = rbf(out[f] * r1);
    out[DH + f] = rbf(out[DH + f] * r2);
  }
}

__global__ __launch_bounds__(256) void kv_kernel(const u16* __restrict__ qkv, const u16* __restrict__ wphi_g,
                                                 float* __restrict__ part) {
  __shared__ float wphi[DH * DH];
  __shared__ float phi[256][37];
  __shared__ float vv[256][19];
  const int h = blockIdx.y, ch = blockIdx.x, tid = threadIdx.x;
  for (int i = tid; i < DH * DH; i += 256) wphi[i] = bf2f(wphi_g[h * DH * DH + i]);
  __syncthreads();
  const int t = ch * 256 + tid;
  float kk[DH], ph[36];
#pragma unroll
  for (int d = 0; d < DH; ++d) {
    kk[d] = bf2f(qkv[(size_t)t * N3 + D_ + h * DH + d]);
    vv[tid][d] = bf2f(qkv[(size_t)t * N3 + 2 * D_ + h * DH + d]);
  }
  vv[tid][18] = 1.f;
  hedgehog(kk, wphi, ph);
#pragma unroll
  for (int f = 0; f < 36; ++f) phi[tid][f] = ph[f];
  __syncthreads();
  for (int o = tid; o < 36 * 19; o += 256) {
    const int f = o / 19, d = o % 19;
    float s = 0.f;
    for (int i = 0; i < 256; ++i) s = fmaf(phi[i][f], vv[i][d], s);
    part[((size_t)h * 8 + ch) * 684 + o] = s;
  }
}

__global__ __launch_bounds__(256) void lin_o_kernel(const u16* __restrict__ qkv, const u16* __restrict__ wphi_g,
                                                    const float* __restrict__ part, u16* __restrict__ o) {
  __shared__ float wphi[DH * DH];
  __shared__ float kv[36 * 19];
  const int h = blockIdx.y, ch = blockIdx.x, tid = threadIdx.x;
  for (int i = tid; i < DH * DH; i += 256) wphi[i] = bf2f(wphi_g[h * DH * DH + i]);
  for (int i = tid; i < 684; i += 256) {
    float s = 0.f;
#pragma unroll
    for (int c = 0; c < 8; ++c) s += part[((size_t)h * 8 + c) * 684 + i];
    kv[i] = rbf(s);
  }
  __syncthreads();
  const int t = ch * 256 + tid;
  float qq[DH], ph[36];
#pragma unroll
  for (int d = 0; d < DH; ++d) qq[d] = bf2f(qkv[(size_t)t * N3 + h * DH + d]);
  hedgehog(qq, wphi, ph);
  float den = 0.f;
#pragma unroll
  for (int f = 0; f < 36; ++f) den = fmaf(ph[f], kv[f * 19 + 18], den);
  den = rbf(rbf(den) + 1e-6f);
#pragma unroll
  for (int d = 0; d < DH; ++d) {
    float n = 0.f;
#pragma unroll
    for (int f = 0; f < 36; ++f) n = fmaf(ph[f], kv[f * 19 + d], n);
    o[(size_t)t * D_ + h * DH + d] = f2bf(rbf(n) / den);
  }
}

constexpr int KT = 256;
constexpr int VSTR = KT + 8;
__global__ __launch_bounds__(512, 2) void attn_kernel(const u16* __restrict__ Qh, const u16* __restrict__ Kh,
                                                      const u16* __restrict__ Vg, u16* __restrict__ o) {
  __shared__ __attribute__((aligned(16))) u16 Ks[2][KT * 32];
  __shared__ __attribute__((aligned(16))) u16 Vt[2][32 * VSTR];
  const int h = blockIdx.y, tid = threadIdx.x, lane = tid & 63, wid = tid >> 6;
  const int q0 = blockIdx.x * 256 + wid * 32;
  const int l31 = lane & 31, hh = lane >> 5;
  for (int i = tid; i < 2 * 14 * VSTR; i += 512) {
    const int b = i / (14 * VSTR), k = i - b * 14 * VSTR;
    Vt[b][18 * VSTR + k] = (k < VSTR) ? (u16)0x3f80 : (u16)0;
  }
  const u32x4* Kg4 = reinterpret_cast<const u32x4*>(Kh + (size_t)h * S_ * 32);
  const u32x4* Vg4 = reinterpret_cast<const u32x4*>(Vg + (size_t)h * 32 * S_);
  const int kkey = tid >> 1, kc = (tid & 1) * 2;
  const int vrow0 = tid >> 5, vch = tid & 31;
  u32x4 kr0, kr1, vr0, vr1;
  auto gload = [&](int kt) {
    kr0 = Kg4[(kt * KT + kkey) * 4 + kc];
    if (kc == 0) kr1 = Kg4[(kt * KT + kkey) * 4 + 1];
    vr0 = Vg4[(size_t)vrow0 * (S_ / 8) + kt * (KT / 8) + vch];
    if (tid < 64) vr1 = Vg4[(size_t)(16 + vrow0) * (S_ / 8) + kt * (KT / 8) + vch];
  };
  auto swrite = [&](int b) {
    const int sw = (kkey >> 2) & 3;
    if (kc == 0) {
      *reinterpret_cast<u32x4*>(&Ks[b][kkey * 32 + ((0 ^ sw) * 8)]) = kr0;
      *reinterpret_cast<u32x4*>(&Ks[b][kkey * 32 + ((1 ^ sw) * 8)]) = kr1;
    } else {
      u32x4 a = {kr0[0], 0u, 0u, 0u}, z = {0u, 0u, 0u, 0u};
      *reinterpret_cast<u32x4*>(&Ks[b][kkey * 32 + ((2 ^ sw) * 8)]) = a;
      *reinterpret_cast<u32x4*>(&Ks[b][kkey * 32 + ((3 ^ sw) * 8)]) = z;
    }
    *reinterpret_cast<u32x4*>(&Vt[b][vrow0 * VSTR + vch * 8]) = vr0;
    if (tid < 64) *reinterpret_cast<u32x4*>(&Vt[b][(16 + vrow0) * VSTR + vch * 8]) = vr1;
  };
  gload(0);
  const float qs = 0.23570226039551584f * 1.4426950408889634f;
  bf16x8 qf[2];
  {
    const u32x4* qp = reinterpret_cast<const u32x4*>(Qh + ((size_t)h * S_ + q0 + l31) * 32);
    const u32x4 a = qp[hh], b = qp[2];
    u32x4 w0, w1;
#pragma unroll
    for (int j = 0; j < 4; ++j) w0[j] = pack2(lo16(a[j]) * qs, hi16(a[j]) * qs);
    w1[0] = hh == 0 ? pack2(lo16(b[0]) * qs, hi16(b[0]) * qs) : 0u;
    w1[1] = 0u; w1[2] = 0u; w1[3] = 0u;
    qf[0] = __builtin_bit_cast(bf16x8, w0);
    qf[1] = __builtin_bit_cast(bf16x8, w1);
  }
  swrite(0);
  f32x16 oacc, cm;
#pragma unroll
  for (int r = 0; r < 16; ++r) { oacc[r] = 0.f; cm[r] = 0.f; }
  const int koff0 = l31 * 32 + (((0 + hh) ^ ((l31 >> 2) & 3)) * 8);
  const int koff1 = l31 * 32 + (((2 + hh) ^ ((l31 >> 2) & 3)) * 8);
  const int voff = l31 * VSTR + hh * 8;
  auto qk = [&](int b, int t) {
    const bf16x8 k0 = *reinterpret_cast<const bf16x8*>(&Ks[b][koff0 + t * 32 * 32]);
    const bf16x8 k1 = *reinterpret_cast<const bf16x8*>(&Ks[b][koff1 + t * 32 * 32]);
    f32x16 s = __builtin_amdgcn_mfma_f32_32x32x16_bf16(k0, qf[0], cm, 0, 0, 0);
    return __builtin_amdgcn_mfma_f32_32x32x16_bf16(k1, qf[1], s, 0, 0, 0);
  };
  auto sm_pv = [&](f32x16& s, f32x16& sn, int b, int t) {
    int im = max(__float_as_int(s[0]), __float_as_int(s[1]));
#pragma unroll
    for (int r = 2; r < 16; r += 2) im = max(im, max(__float_as_int(s[r]), __float_as_int(s[r + 1])));
    {
      auto rr = __builtin_amdgcn_permlane32_swap((unsigned)im, (unsigned)im, false, false);
      im = max((int)rr[0], (int)rr[1]);
    }
    if (__any(im > 0)) {
      const float delta = __int_as_float(max(im, 0));
      const float sc = __builtin_amdgcn_exp2f(-delta);
#pragma unroll
      for (int r = 0; r < 16; ++r) {
        s[r] -= delta;
        sn[r] -= delta;
        cm[r] -= delta;
        oacc[r] *= sc;
      }
    }
    bf16x8 pf[2];
#pragma unroll
    for (int ss = 0; ss < 2; ++ss) {
      u32x4 w;
#pragma unroll
      for (int j = 0; j < 4; ++j)
        w[j] = pack2(__builtin_amdgcn_exp2f(s[8 * ss + 2 * j]), __builtin_amdgcn_exp2f(s[8 * ss + 2 * j + 1]));
      pf[ss] = __builtin_bit_cast(bf16x8, w);
    }
#pragma unroll
    for (int ss = 0; ss < 2; ++ss) {
      const bf16x8 vf = *reinterpret_cast<const bf16x8*>(&Vt[b][voff + t * 32 + ss * 16]);
      oacc = __builtin_amdgcn_mfma_f32_32x32x16_bf16(vf, pf[ss], oacc, 0, 0, 0);
    }
  };
  __syncthreads();
  f32x16 sA = qk(0, 0), sB;
  {
    float tmax = sA[0];
#pragma unroll
    for (int r = 1; r < 16; ++r) tmax = fmaxf(tmax, sA[r]);
    tmax = fmaxf(tmax, __shfl_xor(tmax, 32));
#pragma unroll
    for (int r = 0; r < 16; ++r) { sA[r] -= tmax; cm[r] = -tmax; }
  }
  for (int kt = 0; kt < S_ / KT; ++kt) {
    const int b = kt & 1;
    const bool more = kt + 1 < S_ / KT;
    if (more) gload(kt + 1);
#pragma unroll
    for (int t = 0; t < KT / 32; t += 2) {
      sB = qk(b, t + 1);
      sm_pv(sA, sB, b, t);
      if (t + 2 < KT / 32) {
        sA = qk(b, t + 2);
        sm_pv(sB, sA, b, t + 1);
      } else {
        if (more) {
          swrite(b ^ 1);
          __syncthreads();
          sA = qk(b ^ 1, 0);
        } else {
#pragma unroll
          for (int r = 0; r < 16; ++r) sA[r] = 0.f;
        }
        sm_pv(sB, sA, b, t + 1);
      }
    }
    __syncthreads();
  }
  const float l = __shfl(oacc[10], l31);
  const float rl = 1.f / l;
  u16* op = o + (size_t)(q0 + l31) * D_ + h * DH;
#pragma unroll
  for (int r = 0; r < 16; ++r) {
    const int d = (r & 3) + 8 * (r >> 2) + 4 * hh;
    if (d < DH) op[d] = f2bf(oacc[r] * rl);
  }
}

extern "C" void kernel_launch(void* const* d_in, const int* in_sizes, int n_in, void* d_out, int out_size,
                              void* d_ws, size_t ws_size, hipStream_t stream) {
  const u16* A_x = (const u16*)d_in[0];
  const u16* A_wqkv = (const u16*)d_in[1];
  const u16* A_wphi = (const u16*)d_in[2];
  const u16* A_wo = (const u16*)d_in[3];
  const u16* B_wqkv = (const u16*)d_in[5];
  const u16* B_wo = (const u16*)d_in[6];
  char* ws = (char*)d_ws;
  size_t off = 0;
  auto alloc = [&](size_t bytes) { void* p = ws + off; off += (bytes + 255) & ~(size_t)255; return p; };
  u16* PAq = (u16*)alloc((size_t)D_ * N3 * 2);
  u16* PBq = (u16*)alloc((size_t)D_ * N3 * 2);
  u16* PAo = (u16*)alloc((size_t)D_ * D_ * 2);
  u16* PBo = (u16*)alloc((size_t)D_ * D_ * 2);
  u16* qkv = (u16*)alloc((size_t)3 * H_ * S_ * 32 * 2);
  u16* ob = (u16*)alloc((size_t)S_ * D_ * 2);
  u16* x1 = (u16*)alloc((size_t)S_ * D_ * 2);
  u16* x2 = (u16*)alloc((size_t)S_ * D_ * 2);
  u16* x3 = (u16*)alloc((size_t)S_ * D_ * 2);
  float* part = (float*)alloc((size_t)H_ * 8 * 684 * 4);

  float* csum = (float*)alloc((size_t)2 * N3 * 4);
  pack_w_kernel<<<dim3(108 * 18 * 64 / 256, 6), 256, 0, stream>>>(A_wqkv, B_wqkv, A_wo, B_wo, PAq, PBq, PAo, PBo, csum);

  const u16* xin = A_x;
  u16* xs[4] = {x1, x2, x3, (u16*)d_out};
  for (int layer = 0; layer < 4; ++layer) {
    const bool isA = (layer & 1) == 0;
    if (isA) {
      gemm_kernel<3, 0><<<dim3(9, 32), 512, 0, stream>>>(xin, PAq, nullptr, csum, qkv);
      kv_kernel<<<dim3(8, 32), 256, 0, stream>>>(qkv, A_wphi, part);
      lin_o_kernel<<<dim3(8, 32), 256, 0, stream>>>(qkv, A_wphi, part, ob);
    } else {
      gemm_kernel<3, 1><<<dim3(9, 32), 512, 0, stream>>>(xin, PBq, nullptr, csum + N3, qkv);
      attn_kernel<<<dim3(8, 32), 512, 0, stream>>>(qkv, qkv + (size_t)H_ * S_ * 32, qkv + (size_t)2 * H_ * S_ * 32, ob);
    }
    gemm_kernel<1, 2><<<dim3(9, 32), 512, 0, stream>>>(ob, isA ? PAo : PBo, xin, nullptr, xs[layer]);
    xin = xs[layer];
  }
}
```

```cpp
#include <hip/hip_runtime.h>
#include <hip/hip_bf16.h>
#include <stdint.h>
#include <type_traits>

typedef __attribute__((ext_vector_type(8))) short bf16x8;
typedef __attribute__((ext_vector_type(4))) float f32x4;
typedef __attribute__((ext_vector_type(16))) float f32x16;
typedef __attribute__((ext_vector_type(4))) unsigned u32x4;
typedef __attribute__((ext_vector_type(2))) unsigned u32x2;
typedef unsigned short u16;

constexpr int S_ = 2048, D_ = 576, H_ = 32, DH = 18, N3 = 1728;

__device__ __forceinline__ float bf2f(u16 v) { return __uint_as_float(((unsigned)v) << 16); }
__device__ __forceinline__ u16 f2bf(float f) {
  __hip_bfloat16 b = __float2bfloat16(f);
  return *reinterpret_cast<u16*>(&b);
}
__device__ __forceinline__ float rbf(float f) { return bf2f(f2bf(f)); }
typedef __attribute__((ext_vector_type(2))) __bf16 bf16x2_t;
typedef __attribute__((ext_vector_type(2))) float f32x2;
__device__ __forceinline__ unsigned pack2(float lo, float hi) {
  f32x2 f = {lo, hi};
  return __builtin_bit_cast(unsigned, __builtin_convertvector(f, bf16x2_t));
}
__device__ __forceinline__ float lo16(unsigned v) { return __uint_as_float(v << 16); }
__device__ __forceinline__ float hi16(unsigned v) { return __uint_as_float(v & 0xffff0000u); }

__global__ __launch_bounds__(256) void pack_w_kernel(const u16* __restrict__ Wa, const u16* __restrict__ Wb,
                                                     const u16* __restrict__ Wc, const u16* __restrict__ Wd,
                                                     u16* __restrict__ Pa, u16* __restrict__ Pb,
                                                     u16* __restrict__ Pc, u16* __restrict__ Pd,
                                                     float* __restrict__ csum) {
  const int which = blockIdx.y;
  if (which >= 4) {
    __shared__ float red[4][64];
    if (blockIdx.x >= 27) return;
    const u16* W = which == 4 ? Wa : Wb;
    const int c = threadIdx.x & 63, g = threadIdx.x >> 6;
    const int n = blockIdx.x * 64 + c;
    float sum = 0.f;
#pragma unroll 16
    for (int k = 0; k < 144; ++k) sum += bf2f(W[(size_t)(g * 144 + k) * N3 + n]);
    red[g][c] = sum;
    __syncthreads();
    if (g == 0) csum[(which - 4) * N3 + n] = (red[0][c] + red[1][c]) + (red[2][c] + red[3][c]);
    return;
  }
  const u16* W = which == 0 ? Wa : which == 1 ? Wb : which == 2 ? Wc : Wd;
  u16* P = which == 0 ? Pa : which == 1 ? Pb : which == 2 ? Pc : Pd;
  const int N = which < 2 ? N3 : D_;
  const int idx = blockIdx.x * 256 + threadIdx.x;
  const int lane = idx & 63, t = idx >> 6;
  const int kb = t % 18, nb = t / 18;
  if (nb >= N / 16) return;
  const int k0 = kb * 32 + 8 * (lane >> 4), n = nb * 16 + (lane & 15);
  u16 v[8];
#pragma unroll
  for (int j = 0; j < 8; ++j) v[j] = W[(size_t)(k0 + j) * N + n];
  u32x4 o;
#pragma unroll
  for (int j = 0; j < 4; ++j) o[j] = (unsigned)v[2 * j] | ((unsigned)v[2 * j + 1] << 16);
  *reinterpret_cast<u32x4*>(P + ((size_t)(nb * 18 + kb) * 64 + lane) * 8) = o;
}

template <int NCG, int NCB, int MODE>
__global__ __launch_bounds__(NCG * 128, 4) void gemm_kernel(const u16* __restrict__ A, const u16* __restrict__ Wp,
                                                      const u16* __restrict__ R, const float* __restrict__ csum,
                                                      u16* __restrict__ C) {
  __shared__ __attribute__((aligned(16))) u16 As[64 * 72 * 8];
  __shared__ float stats[2][64][2];
  constexpr int PF = NCB == 1 ? 4 : (MODE == 1 ? 2 : 3);
  constexpr bool LN = MODE != 2;
  const int tid = threadIdx.x, lane = tid & 63, wid = tid >> 6;
  const int cg = wid % NCG, kh = wid / NCG;
  constexpr int NCT = (MODE == 2 ? D_ : N3) / (16 * NCG * NCB);
  const int bidx = blockIdx.x >> 3, xcd = blockIdx.x & 7;
  const int row0 = (xcd * 4 + bidx / NCT) * 64;
  const int nb0 = (bidx % NCT) * (NCG * NCB) + cg * NCB;
  const bf16x8* Bp = reinterpret_cast<const bf16x8*>(Wp) + ((size_t)nb0 * 18 + kh * 9) * 64 + lane;
  bf16x8 bq[PF][NCB];
#pragma unroll
  for (int s = 0; s < PF; ++s)
#pragma unroll
    for (int j = 0; j < NCB; ++j) bq[s][j] = Bp[(j * 18 + s) * 64];
  {
    constexpr int NT = NCG * 128, NI = 64 * 72 / NT;
    const u32x4* src = reinterpret_cast<const u32x4*>(A + (size_t)row0 * D_);
    u32x4 v[NI];
#pragma unroll
    for (int i = 0; i < NI; ++i) v[i] = src[i * NT + tid];
#pragma unroll
    for (int i = 0; i < NI; ++i) {
      const int idx = i * NT + tid;
      const int r = idx / 72, ch = idx - r * 72;
      *reinterpret_cast<u32x4*>(As + (r * 72 + (ch ^ ((r >> 1) & 7))) * 8) = v[i];
    }
  }
  __syncthreads();
  const int aoff = (lane & 15) * 72 * 8, asw = ((lane & 15) >> 1) & 7;
  const bool vblk = (MODE == 1) && (nb0 * 16 >= 2 * D_);
  f32x4 acc[4][NCB];
#pragma unroll
  for (int i = 0; i < 4; ++i)
#pragma unroll
    for (int j = 0; j < NCB; ++j) acc[i][j] = f32x4{0.f, 0.f, 0.f, 0.f};
  float su[4] = {0.f, 0.f, 0.f, 0.f}, sq[4] = {0.f, 0.f, 0.f, 0.f}, shift = 0.f;
  if (LN) shift = bf2f(As[(cg * 16 + (lane & 15)) * 72 * 8 + (asw * 8)]);
  auto kloop = [&](auto swapped) {
#pragma unroll
    for (int kk = 0; kk < 9; ++kk) {
      const int ks = kh * 9 + kk;
      bf16x8 b[NCB];
#pragma unroll
      for (int j = 0; j < NCB; ++j) b[j] = bq[kk % PF][j];
      if (kk + PF < 9) {
#pragma unroll
        for (int j = 0; j < NCB; ++j) bq[kk % PF][j] = Bp[(j * 18 + kk + PF) * 64];
      }
#pragma unroll
      for (int i = 0; i < 4; ++i) {
        const bf16x8 a =
            *reinterpret_cast<const bf16x8*>(As + aoff + (i * 16 * 72 + ((ks * 4 + (lane >> 4)) ^ asw)) * 8);
        if (LN && i == cg) {
          const u32x4 w = __builtin_bit_cast(u32x4, a);
#pragma unroll
          for (int e = 0; e < 4; ++e) {
            const float d0 = lo16(w[e]) - shift, d1 = hi16(w[e]) - shift;
            su[e] += d0 + d1;
            sq[e] = fmaf(d0, d0, fmaf(d1, d1, sq[e]));
          }
        }
#pragma unroll
        for (int j = 0; j < NCB; ++j) {
          if (decltype(swapped)::value)
            acc[i][j] = __builtin_amdgcn_mfma_f32_16x16x32_bf16(b[j], a, acc[i][j], 0, 0, 0);
          else
            acc[i][j] = __builtin_amdgcn_mfma_f32_16x16x32_bf16(a, b[j], acc[i][j], 0, 0, 0);
        }
      }
    }
  };
  if (!vblk) kloop(std::integral_constant<bool, true>{});
  else kloop(std::integral_constant<bool, false>{});
  if (LN) {
    float ssum = (su[0] + su[1]) + (su[2] + su[3]), ssq = (sq[0] + sq[1]) + (sq[2] + sq[3]);
    ssum += __shfl_xor(ssum, 16);
    ssq += __shfl_xor(ssq, 16);
    ssum += __shfl_xor(ssum, 32);
    ssq += __shfl_xor(ssq, 32);
    if (lane < 16) {
      stats[kh][cg * 16 + lane][0] = ssum;
      stats[kh][cg * 16 + lane][1] = ssq;
    }
  }
  __syncthreads();
  float* red = reinterpret_cast<float*>(As);
  if (kh == 1) {
#pragma unroll
    for (int i = 0; i < 4; ++i)
#pragma unroll
      for (int j = 0; j < NCB; ++j)
        *reinterpret_cast<f32x4*>(red + (((cg * 4 + i) * NCB + j) * 64 + lane) * 4) = acc[i][j];
  } else if (LN) {
    if (cg == 0) {
      const float s1 = stats[0][lane][0] + stats[1][lane][0];
      const float s2 = stats[0][lane][1] + stats[1][lane][1];
      const float dm = s1 * (1.0f / D_);
      const float sh = bf2f(A[(size_t)(row0 + lane) * D_]);
      const float mean = sh + dm;
      const float var = rbf(fmaxf(s2 * (1.0f / D_) - dm * dm, 0.f));
      const float sd = rbf(sqrtf(rbf(var + 1e-5f)));
      stats[0][lane][0] = rbf(mean);
      stats[0][lane][1] = 1.0f / sd;
    }
  }
  __syncthreads();
  if (kh == 1) return;
#pragma unroll
  for (int i = 0; i < 4; ++i)
#pragma unroll
    for (int j = 0; j < NCB; ++j) {
      const f32x4 o = *reinterpret_cast<const f32x4*>(red + (((cg * 4 + i) * NCB + j) * 64 + lane) * 4);
      f32x4 v = acc[i][j] + o;
      if (MODE == 2) {
        const int t = row0 + i * 16 + (lane & 15);
        const int n = (nb0 + j) * 16 + (lane >> 4) * 4;
        const u32x2 rr = *reinterpret_cast<const u32x2*>(R + (size_t)t * D_ + n);
        const unsigned p0 = pack2(v[0], v[1]), p1 = pack2(v[2], v[3]);
        u32x2 w = {pack2(lo16(p0) + lo16(rr[0]), hi16(p0) + hi16(rr[0])),
                   pack2(lo16(p1) + lo16(rr[1]), hi16(p1) + hi16(rr[1]))};
        *reinterpret_cast<u32x2*>(C + (size_t)t * D_ + n) = w;
      } else if (!vblk) {
        const int tl = i * 16 + (lane & 15);
        const int t = row0 + tl;
        const int n = (nb0 + j) * 16 + (lane >> 4) * 4;
        const float mb = stats[0][tl][0], rs = stats[0][tl][1];
        const f32x4 cs = *reinterpret_cast<const f32x4*>(csum + n);
#pragma unroll
        for (int e = 0; e < 4; ++e) v[e] = (v[e] - mb * cs[e]) * rs;
        if (MODE == 0) {
          u32x2 w = {pack2(v[0], v[1]), pack2(v[2], v[3])};
          *reinterpret_cast<u32x2*>(C + (size_t)t * N3 + n) = w;
        } else {
          const int which = n >= D_;
          const int c0 = n - which * D_;
          const int h0 = c0 / DH, d0 = c0 - h0 * DH;
          const int c1 = c0 + 2;
          const int h1 = c1 / DH, d1 = c1 - h1 * DH;
          u16* base = C + (size_t)which * (H_ * S_ * 32);
          *reinterpret_cast<unsigned*>(base + ((size_t)h0 * S_ + t) * 32 + d0) = pack2(v[0], v[1]);
          *reinterpret_cast<unsigned*>(base + ((size_t)h1 * S_ + t) * 32 + d1) = pack2(v[2], v[3]);
        }
      } else {
        const int n = (nb0 + j) * 16 + (lane & 15);
        const int c = n - 2 * D_;
        const int h = c / DH, d = c - h * DH;
        const int g = lane >> 4;
        const float cs = csum[n];
#pragma unroll
        for (int e = 0; e < 4; ++e) {
          const int tl = i * 16 + g * 4 + e;
          v[e] = (v[e] - stats[0][tl][0] * cs) * stats[0][tl][1];
        }
        const int pos = row0 + i * 16 + (g & 1) * 8 + (g >> 1) * 4;
        u16* base = C + (size_t)2 * (H_ * S_ * 32);
        u32x2 w = {pack2(v[0], v[1]), pack2(v[2], v[3])};
        *reinterpret_cast<u32x2*>(base + ((size_t)h * 32 + d) * S_ + pos) = w;
      }
    }
}

__device__ __forceinline__ void hedgehog(const unsigned* w, const float* __restrict__ wphi, float* out) {
  float p[20];
#pragma unroll
  for (int f = 0; f < 20; ++f) p[f] = 0.f;
#pragma unroll
  for (int d = 0; d < DH; ++d) {
    const float x = (d & 1) ? hi16(w[d >> 1]) : lo16(w[d >> 1]);
#pragma unroll
    for (int f4 = 0; f4 < 5; ++f4) {
      const f32x4 ww = *reinterpret_cast<const f32x4*>(wphi + d * 20 + f4 * 4);
#pragma unroll
      for (int e = 0; e < 4; ++e) p[f4 * 4 + e] = fmaf(x, ww[e], p[f4 * 4 + e]);
    }
  }
  float mx = -INFINITY, mn = INFINITY;
#pragma unroll
  for (int f = 0; f < DH; ++f) {
    p[f] = rbf(p[f]);
    mx = fmaxf(mx, p[f]);
    mn = fminf(mn, p[f]);
  }
  const float L2E = 1.4426950408889634f;
  float s1 = 0.f, s2 = 0.f;
#pragma unroll
  for (int f = 0; f < DH; ++f) {
    out[f] = __builtin_amdgcn_exp2f((p[f] - mx) * L2E);
    out[DH + f] = __builtin_amdgcn_exp2f((mn - p[f]) * L2E);
    s1 += out[f];
    s2 += out[DH + f];
  }
  const float r1 = 1.f / s1, r2 = 1.f / s2;
#pragma unroll
  for (int f = 0; f < DH; ++f) {
    out[f] = rbf(out[f] * r1);
    out[DH + f] = rbf(out[DH + f] * r2);
  }
}

constexpr int NCH = 16;
constexpr int CT = S_ / NCH;
constexpr int PSTR = CT + 8;
__global__ __launch_bounds__(128) void kv_kernel(const u16* __restrict__ Kh, const u16* __restrict__ Vg,
                                                 const u16* __restrict__ wphi_g, float* __restrict__ part) {
  __shared__ __attribute__((aligned(16))) float wphi[DH * 20];
  __shared__ __attribute__((aligned(16))) u16 phiT[48 * PSTR];
  const int tid = threadIdx.x, lane = tid & 63, wid = tid >> 6;
  const int h = (blockIdx.x & 7) * 4 + (blockIdx.x >> 7), ch = (blockIdx.x >> 3) & 15;
  const int t0 = ch * CT;
  const u32x4* kp = reinterpret_cast<const u32x4*>(Kh + ((size_t)h * S_ + t0 + tid) * 32);
  const u32x4 k0 = kp[0], k1 = kp[1], k2 = kp[2];
  for (int i = tid; i < DH * 20; i += 128) {
    const int d = i / 20, f = i - d * 20;
    wphi[i] = f < DH ? bf2f(wphi_g[(h * DH + d) * DH + f]) : 0.f;
  }
  for (int i = tid; i < 12 * PSTR / 2; i += 128) reinterpret_cast<unsigned*>(phiT + 36 * PSTR)[i] = 0u;
  bf16x8 vb[4][2];
  {
    const int n = lane & 15, g = lane >> 4;
#pragma unroll
    for (int ks = 0; ks < 4; ++ks) {
      vb[ks][0] = *reinterpret_cast<const bf16x8*>(Vg + ((size_t)h * 32 + n) * S_ + t0 + ks * 32 + g * 8);
      const bf16x8 x = *reinterpret_cast<const bf16x8*>(Vg + ((size_t)h * 32 + 16 + (n & 1)) * S_ + t0 + ks * 32 + g * 8);
      const short c = n == 2 ? (short)0x3f80 : (short)0;
#pragma unroll
      for (int j = 0; j < 8; ++j) vb[ks][1][j] = n < 2 ? x[j] : c;
    }
  }
  __syncthreads();
  {
    const unsigned w[9] = {k0[0], k0[1], k0[2], k0[3], k1[0], k1[1], k1[2], k1[3], k2[0]};
    float ph[36];
    hedgehog(w, wphi, ph);
    const int k16 = tid & 15;
    const int pos = (tid & ~15) + ((k16 >> 2) & 1) * 8 + (k16 & 3) + 4 * (k16 >> 3);
#pragma unroll
    for (int f = 0; f < 36; ++f) phiT[f * PSTR + pos] = f2bf(ph[f]);
  }
  __syncthreads();
#pragma unroll
  for (int fb = 0; fb < 3; ++fb) {
    f32x4 acc = {0.f, 0.f, 0.f, 0.f};
#pragma unroll
    for (int ks = 0; ks < 4; ++ks) {
      const bf16x8 a = *reinterpret_cast<const bf16x8*>(phiT + (fb * 16 + (lane & 15)) * PSTR + ks * 32 + (lane >> 4) * 8);
      acc = __builtin_amdgcn_mfma_f32_16x16x32_bf16(a, wid == 0 ? vb[ks][0] : vb[ks][1], acc, 0, 0, 0);
    }
    float* dst = part + ((size_t)(h * NCH + ch) * 48 + fb * 16 + (lane >> 4) * 4) * 32 + wid * 16 + (lane & 15);
#pragma unroll
    for (int r = 0; r < 4; ++r) dst[r * 32] = acc[r];
  }
}

__global__ __launch_bounds__(128) void lin_o_kernel(const u16* __restrict__ Qh, const u16* __restrict__ wphi_g,
                                                    const float* __restrict__ part, u16* __restrict__ o) {
  __shared__ __attribute__((aligned(16))) float wphi[DH * 20];
  __shared__ __attribute__((aligned(16))) float kv[36 * 20];
  const int tid = threadIdx.x;
  const int h = (blockIdx.x & 7) * 4 + (blockIdx.x >> 7), ch = (blockIdx.x >> 3) & 15;
  const int t = ch * CT + tid;
  const u32x4* qp = reinterpret_cast<const u32x4*>(Qh + ((size_t)h * S_ + t) * 32);
  const u32x4 q0 = qp[0], q1 = qp[1], q2 = qp[2];
  for (int i = tid; i < DH * 20; i += 128) {
    const int d = i / 20, f = i - d * 20;
    wphi[i] = f < DH ? bf2f(wphi_g[(h * DH + d) * DH + f]) : 0.f;
  }
  for (int i = tid; i < 36 * 20; i += 128) {
    const int f = i / 20, d = i - f * 20;
    float s = 0.f;
#pragma unroll
    for (int c = 0; c < NCH; ++c) s += part[((size_t)(h * NCH + c) * 48 + f) * 32 + d];
    kv[i] = rbf(s);
  }
  __syncthreads();
  const unsigned w[9] = {q0[0], q0[1], q0[2], q0[3], q1[0], q1[1], q1[2], q1[3], q2[0]};
  float ph[36];
  hedgehog(w, wphi, ph);
  float n[20];
#pragma unroll
  for (int d = 0; d < 20; ++d) n[d] = 0.f;
#pragma unroll
  for (int f = 0; f < 36; ++f)
#pragma unroll
    for (int d4 = 0; d4 < 5; ++d4) {
      const f32x4 kk = *reinterpret_cast<const f32x4*>(kv + f * 20 + d4 * 4);
#pragma unroll
      for (int e = 0; e < 4; ++e) n[d4 * 4 + e] = fmaf(ph[f], kk[e], n[d4 * 4 + e]);
    }
  const float den = rbf(rbf(n[18]) + 1e-6f);
  const float rd = 1.0f / den;
  unsigned* op = reinterpret_cast<unsigned*>(o + (size_t)t * D_ + h * DH);
#pragma unroll
  for (int d = 0; d < DH; d += 2) op[d >> 1] = pack2(rbf(n[d]) * rd, rbf(n[d + 1]) * rd);
}

constexpr int KT = 256;
constexpr int VSTR = KT + 8;
__global__ __launch_bounds__(512, 2) void attn_kernel(const u16* __restrict__ Qh, const u16* __restrict__ Kh,
                                                      const u16* __restrict__ Vg, u16* __restrict__ o) {
  __shared__ __attribute__((aligned(16))) u16 Ks[2][KT * 32];
  __shared__ __attribute__((aligned(16))) u16 Vt[2][32 * VSTR];
  const int tid = threadIdx.x, lane = tid & 63, wid = tid >> 6;
  const int h = (blockIdx.x & 7) * 4 + (blockIdx.x >> 6);
  const int q0 = ((blockIdx.x >> 3) & 7) * 256 + wid * 32;
  const int l31 = lane & 31, hh = lane >> 5;
  for (int i = tid; i < 2 * 14 * VSTR; i += 512) {
    const int b = i / (14 * VSTR), k = i - b * 14 * VSTR;
    Vt[b][18 * VSTR + k] = (k < VSTR) ? (u16)0x3f80 : (u16)0;
  }
  const u32x4* Kg4 = reinterpret_cast<const u32x4*>(Kh + (size_t)h * S_ * 32);
  const u32x4* Vg4 = reinterpret_cast<const u32x4*>(Vg + (size_t)h * 32 * S_);
  const int kkey = tid >> 1, kc = (tid & 1) * 2;
  const int vrow0 = tid >> 5, vch = tid & 31;
  u32x4 kr0, kr1, vr0, vr1;
  auto gload = [&](int kt) {
    kr0 = Kg4[(kt * KT + kkey) * 4 + kc];
    if (kc == 0) kr1 = Kg4[(kt * KT + kkey) * 4 + 1];
    vr0 = Vg4[(size_t)vrow0 * (S_ / 8) + kt * (KT / 8) + vch];
    if (tid < 64) vr1 = Vg4[(size_t)(16 + vrow0) * (S_ / 8) + kt * (KT / 8) + vch];
  };
  auto swrite = [&](int b) {
    const int sw = (kkey >> 2) & 3;
    if (kc == 0) {
      *reinterpret_cast<u32x4*>(&Ks[b][kkey * 32 + ((0 ^ sw) * 8)]) = kr0;
      *reinterpret_cast<u32x4*>(&Ks[b][kkey * 32 + ((1 ^ sw) * 8)]) = kr1;
    } else {
      u32x4 a = {kr0[0], 0u, 0u, 0u}, z = {0u, 0u, 0u, 0u};
      *reinterpret_cast<u32x4*>(&Ks[b][kkey * 32 + ((2 ^ sw) * 8)]) = a;
      *reinterpret_cast<u32x4*>(&Ks[b][kkey * 32 + ((3 ^ sw) * 8)]) = z;
    }
    *reinterpret_cast<u32x4*>(&Vt[b][vrow0 * VSTR + vch * 8]) = vr0;
    if (tid < 64) *reinterpret_cast<u32x4*>(&Vt[b][(16 + vrow0) * VSTR + vch * 8]) = vr1;
  };
  gload(0);
  const float qs = 0.23570226039551584f * 1.4426950408889634f;
  bf16x8 qf[2];
  {
    const u32x4* qp = reinterpret_cast<const u32x4*>(Qh + ((size_t)h * S_ + q0 + l31) * 32);
    const u32x4 a = qp[hh], b = qp[2];
    u32x4 w0, w1;
#pragma unroll
    for (int j = 0; j < 4; ++j) w0[j] = pack2(lo16(a[j]) * qs, hi16(a[j]) * qs);
    w1[0] = hh == 0 ? pack2(lo16(b[0]) * qs, hi16(b[0]) * qs) : 0u;
    w1[1] = 0u; w1[2] = 0u; w1[3] = 0u;
    qf[0] = __builtin_bit_cast(bf16x8, w0);
    qf[1] = __builtin_bit_cast(bf16x8, w1);
  }
  swrite(0);
  f32x16 oacc, cm;
#pragma unroll
  for (int r = 0; r < 16; ++r) { oacc[r] = 0.f; cm[r] = 0.f; }
  const int koff0 = l31 * 32 + (((0 + hh) ^ ((l31 >> 2) & 3)) * 8);
  const int koff1 = l31 * 32 + (((2 + hh) ^ ((l31 >> 2) & 3)) * 8);
  const int voff = l31 * VSTR + hh * 8;
  auto qk = [&](int b, int t) {
    const bf16x8 k0 = *reinterpret_cast<const bf16x8*>(&Ks[b][koff0 + t * 32 * 32]);
    const bf16x8 k1 = *reinterpret_cast<const bf16x8*>(&Ks[b][koff1 + t * 32 * 32]);
    f32x16 s = __builtin_amdgcn_mfma_f32_32x32x16_bf16(k0, qf[0], cm, 0, 0, 0);
    return __builtin_amdgcn_mfma_f32_32x32x16_bf16(k1, qf[1], s, 0, 0, 0);
  };
  auto sm_pv = [&](f32x16& s, f32x16& sn, int b, int t) {
    int im = max(__float_as_int(s[0]), __float_as_int(s[1]));
#pragma unroll
    for (int r = 2; r < 16; r += 2) im = max(im, max(__float_as_int(s[r]), __float_as_int(s[r + 1])));
    {
      auto rr = __builtin_amdgcn_permlane32_swap((unsigned)im, (unsigned)im, false, false);
      im = max((int)rr[0], (int)rr[1]);
    }
    if (__any(im > 0x41000000)) {
      const float delta = __int_as_float(max(im, 0));
      const float sc = __builtin_amdgcn_exp2f(-delta);
#pragma unroll
      for (int r = 0; r < 16; ++r) {
        s[r] -= delta;
        sn[r] -= delta;
        cm[r] -= delta;
        oacc[r] *= sc;
      }
    }
    bf16x8 pf[2];
#pragma unroll
    for (int ss = 0; ss < 2; ++ss) {
      u32x4 w;
#pragma unroll
      for (int j = 0; j < 4; ++j)
        w[j] = pack2(__builtin_amdgcn_exp2f(s[8 * ss + 2 * j]), __builtin_amdgcn_exp2f(s[8 * ss + 2 * j + 1]));
      pf[ss] = __builtin_bit_cast(bf16x8, w);
    }
#pragma unroll
    for (int ss = 0; ss < 2; ++ss) {
      const bf16x8 vf = *reinterpret_cast<const bf16x8*>(&Vt[b][voff + t * 32 + ss * 16]);
      oacc = __builtin_amdgcn_mfma_f32_32x32x16_bf16(vf, pf[ss], oacc, 0, 0, 0);
    }
  };
  __syncthreads();
  f32x16 sA = qk(0, 0), sB;
  {
    float tmax = sA[0];
#pragma unroll
    for (int r = 1; r < 16; ++r) tmax = fmaxf(tmax, sA[r]);
    tmax = fmaxf(tmax, __shfl_xor(tmax, 32));
#pragma unroll
    for (int r = 0; r < 16; ++r) { sA[r] -= tmax; cm[r] = -tmax; }
  }
  for (int kt = 0; kt < S_ / KT; ++kt) {
    const int b = kt & 1;
    const bool more = kt + 1 < S_ / KT;
    if (more) gload(kt + 1);
#pragma unroll
    for (int t = 0; t < KT / 32; t += 2) {
      sB = qk(b, t + 1);
      sm_pv(sA, sB, b, t);
      if (t + 2 < KT / 32) {
        sA = qk(b, t + 2);
        sm_pv(sB, sA, b, t + 1);
      } else {
        if (more) {
          swrite(b ^ 1);
          __syncthreads();
          sA = qk(b ^ 1, 0);
        } else {
#pragma unroll
          for (int r = 0; r < 16; ++r) sA[r] = 0.f;
        }
        sm_pv(sB, sA, b, t + 1);
      }
    }
    __syncthreads();
  }
  const float l = __shfl(oacc[10], l31);
  const float rl = 1.f / l;
  u16* op = o + (size_t)(q0 + l31) * D_ + h * DH;
#pragma unroll
  for (int r = 0; r < 16; ++r) {
    const int d = (r & 3) + 8 * (r >> 2) + 4 * hh;
    if (d < DH) op[d] = f2bf(oacc[r] * rl);
  }
}

extern "C" void kernel_launch(void* const* d_in, const int* in_sizes, int n_in, void* d_out, int out_size,
                              void* d_ws, size_t ws_size, hipStream_t stream) {
  const u16* A_x = (const u16*)d_in[0];
  const u16* A_wqkv = (const u16*)d_in[1];
  const u16* A_wphi = (const u16*)d_in[2];
  const u16* A_wo = (const u16*)d_in[3];
  const u16* B_wqkv = (const u16*)d_in[5];
  const u16* B_wo = (const u16*)d_in[6];
  char* ws = (char*)d_ws;
  size_t off = 0;
  auto alloc = [&](size_t bytes) { void* p = ws + off; off += (bytes + 255) & ~(size_t)255; return p; };
  u16* PAq = (u16*)alloc((size_t)D_ * N3 * 2);
  u16* PBq = (u16*)alloc((size_t)D_ * N3 * 2);
  u16* PAo = (u16*)alloc((size_t)D_ * D_ * 2);
  u16* PBo = (u16*)alloc((size_t)D_ * D_ * 2);
  u16* qkv = (u16*)alloc((size_t)3 * H_ * S_ * 32 * 2);
  u16* ob = (u16*)alloc((size_t)S_ * D_ * 2);
  u16* x1 = (u16*)alloc((size_t)S_ * D_ * 2);
  u16* x2 = (u16*)alloc((size_t)S_ * D_ * 2);
  u16* x3 = (u16*)alloc((size_t)S_ * D_ * 2);
  float* part = (float*)alloc((size_t)H_ * NCH * 48 * 32 * 4);

  float* csum = (float*)alloc((size_t)2 * N3 * 4);
  pack_w_kernel<<<dim3(108 * 18 * 64 / 256, 6), 256, 0, stream>>>(A_wqkv, B_wqkv, A_wo, B_wo, PAq, PBq, PAo, PBo, csum);

  const u16* xin = A_x;
  u16* xs[4] = {x1, x2, x3, (u16*)d_out};
  for (int layer = 0; layer < 4; ++layer) {
    const bool isA = (layer & 1) == 0;
    if (isA) {
      gemm_kernel<4, 3, 1><<<dim3(9 * 32), 512, 0, stream>>>(xin, PAq, nullptr, csum, qkv);
      kv_kernel<<<dim3(512), 128, 0, stream>>>(qkv + (size_t)H_ * S_ * 32, qkv + (size_t)2 * H_ * S_ * 32, A_wphi, part);
      lin_o_kernel<<<dim3(512), 128, 0, stream>>>(qkv, A_wphi, part, ob);
    } else {
      gemm_kernel<4, 3, 1><<<dim3(9 * 32), 512, 0, stream>>>(xin, PBq, nullptr, csum + N3, qkv);
      attn_kernel<<<dim3(256), 512, 0, stream>>>(qkv, qkv + (size_t)H_ * S_ * 32, qkv + (size_t)2 * H_ * S_ * 32, ob);
    }
    gemm_kernel<4, 1, 2><<<dim3(9 * 32), 512, 0, stream>>>(ob, isA ? PAo : PBo, xin, nullptr, xs[layer]);
    xin = xs[layer];
  }
}
```

```cpp
#include <hip/hip_runtime.h>
#include <hip/hip_bf16.h>
#include <stdint.h>
#include <type_traits>

typedef __attribute__((ext_vector_type(8))) short bf16x8;
typedef __attribute__((ext_vector_type(4))) float f32x4;
typedef __attribute__((ext_vector_type(16))) float f32x16;
typedef __attribute__((ext_vector_type(4))) unsigned u32x4;
typedef __attribute__((ext_vector_type(2))) unsigned u32x2;
typedef unsigned short u16;

constexpr int S_ = 2048, D_ = 576, H_ = 32, DH = 18, N3 = 1728;

__device__ __forceinline__ float bf2f(u16 v) { return __uint_as_float(((unsigned)v) << 16); }
__device__ __forceinline__ u16 f2bf(float f) {
  __hip_bfloat16 b = __float2bfloat16(f);
  return *reinterpret_cast<u16*>(&b);
}
__device__ __forceinline__ float rbf(float f) { return bf2f(f2bf(f)); }
typedef __attribute__((ext_vector_type(2))) __bf16 bf16x2_t;
typedef __attribute__((ext_vector_type(2))) float f32x2;
__device__ __forceinline__ unsigned pack2(float lo, float hi) {
  f32x2 f = {lo, hi};
  return __builtin_bit_cast(unsigned, __builtin_convertvector(f, bf16x2_t));
}
__device__ __forceinline__ float lo16(unsigned v) { return __uint_as_float(v << 16); }
__device__ __forceinline__ float hi16(unsigned v) { return __uint_as_float(v & 0xffff0000u); }

__global__ __launch_bounds__(256) void pack_w_kernel(const u16* __restrict__ Wa, const u16* __restrict__ Wb,
                                                     const u16* __restrict__ Wc, const u16* __restrict__ Wd,
                                                     u16* __restrict__ Pa, u16* __restrict__ Pb,
                                                     u16* __restrict__ Pc, u16* __restrict__ Pd,
                                                     float* __restrict__ csum) {
  const int which = blockIdx.y;
  if (which >= 4) {
    __shared__ float red[16][17];
    if (blockIdx.x >= 108) return;
    const u16* W = which == 4 ? Wa : Wb;
    const int c = threadIdx.x & 15, g = threadIdx.x >> 4;
    const int n = blockIdx.x * 16 + c;
    float sum = 0.f;
#pragma unroll
    for (int k = 0; k < 36; ++k) sum += bf2f(W[(size_t)(g * 36 + k) * N3 + n]);
    red[g][c] = sum;
    __syncthreads();
    if (g == 0) {
      float t = 0.f;
#pragma unroll
      for (int i = 0; i < 16; ++i) t += red[i][c];
      csum[(which - 4) * N3 + n] = t;
    }
    return;
  }
  const u16* W = which == 0 ? Wa : which == 1 ? Wb : which == 2 ? Wc : Wd;
  u16* P = which == 0 ? Pa : which == 1 ? Pb : which == 2 ? Pc : Pd;
  const int N = which < 2 ? N3 : D_;
  const int idx = blockIdx.x * 256 + threadIdx.x;
  const int lane = idx & 63, t = idx >> 6;
  const int kb = t % 18, nb = t / 18;
  if (nb >= N / 16) return;
  const int k0 = kb * 32 + 8 * (lane >> 4), n = nb * 16 + (lane & 15);
  u16 v[8];
#pragma unroll
  for (int j = 0; j < 8; ++j) v[j] = W[(size_t)(k0 + j) * N + n];
  u32x4 o;
#pragma unroll
  for (int j = 0; j < 4; ++j) o[j] = (unsigned)v[2 * j] | ((unsigned)v[2 * j + 1] << 16);
  *reinterpret_cast<u32x4*>(P + ((size_t)(nb * 18 + kb) * 64 + lane) * 8) = o;
}

__global__ __launch_bounds__(512, 4) void gemm_wo_kernel(const u16* __restrict__ A, const u16* __restrict__ Wp,
                                                         const u16* __restrict__ R, u16* __restrict__ C) {
  __shared__ __attribute__((aligned(16))) u16 As[64 * 72 * 8];
  constexpr int PF = 4;
  const int tid = threadIdx.x, lane = tid & 63, wid = tid >> 6;
  const int cg = wid & 3, kh = wid >> 2;
  const int bidx = blockIdx.x >> 3, xcd = blockIdx.x & 7;
  const int row0 = (xcd * 4 + bidx / 9) * 64;
  const int nb0 = (bidx % 9) * 4 + cg;
  const bf16x8* Bp = reinterpret_cast<const bf16x8*>(Wp) + ((size_t)nb0 * 18 + kh * 9) * 64 + lane;
  bf16x8 bq[PF];
#pragma unroll
  for (int s2 = 0; s2 < PF; ++s2) bq[s2] = Bp[s2 * 64];
  u32x2 rr[4];
  if (kh == 0) {
#pragma unroll
    for (int i = 0; i < 4; ++i)
      rr[i] = *reinterpret_cast<const u32x2*>(R + (size_t)(row0 + i * 16 + (lane & 15)) * D_ + nb0 * 16 + (lane >> 4) * 4);
  }
  {
    const u32x4* src = reinterpret_cast<const u32x4*>(A + (size_t)row0 * D_);
    u32x4 v[9];
#pragma unroll
    for (int i = 0; i < 9; ++i) v[i] = src[i * 512 + tid];
#pragma unroll
    for (int i = 0; i < 9; ++i) {
      const int idx = i * 512 + tid;
      const int r = idx / 72, ch = idx - r * 72;
      *reinterpret_cast<u32x4*>(As + (r * 72 + (ch ^ ((r >> 1) & 7))) * 8) = v[i];
    }
  }
  __syncthreads();
  const int aoff = (lane & 15) * 72 * 8, asw = ((lane & 15) >> 1) & 7;
  f32x4 acc[4];
#pragma unroll
  for (int i = 0; i < 4; ++i) acc[i] = f32x4{0.f, 0.f, 0.f, 0.f};
#pragma unroll
  for (int kk = 0; kk < 9; ++kk) {
    const int ks = kh * 9 + kk;
    const bf16x8 b = bq[kk % PF];
    if (kk + PF < 9) bq[kk % PF] = Bp[(kk + PF) * 64];
#pragma unroll
    for (int i = 0; i < 4; ++i) {
      const bf16x8 a = *reinterpret_cast<const bf16x8*>(As + aoff + (i * 16 * 72 + ((ks * 4 + (lane >> 4)) ^ asw)) * 8);
      acc[i] = __builtin_amdgcn_mfma_f32_16x16x32_bf16(b, a, acc[i], 0, 0, 0);
    }
  }
  __syncthreads();
  float* red = reinterpret_cast<float*>(As);
  if (kh == 1) {
#pragma unroll
    for (int i = 0; i < 4; ++i) *reinterpret_cast<f32x4*>(red + ((cg * 4 + i) * 64 + lane) * 4) = acc[i];
  }
  __syncthreads();
  if (kh == 1) return;
#pragma unroll
  for (int i = 0; i < 4; ++i) {
    const f32x4 v = acc[i] + *reinterpret_cast<const f32x4*>(red + ((cg * 4 + i) * 64 + lane) * 4);
    const int t = row0 + i * 16 + (lane & 15);
    const int n = nb0 * 16 + (lane >> 4) * 4;
    const unsigned p0 = pack2(v[0], v[1]), p1 = pack2(v[2], v[3]);
    u32x2 w = {pack2(lo16(p0) + lo16(rr[i][0]), hi16(p0) + hi16(rr[i][0])),
               pack2(lo16(p1) + lo16(rr[i][1]), hi16(p1) + hi16(rr[i][1]))};
    *reinterpret_cast<u32x2*>(C + (size_t)t * D_ + n) = w;
  }
}

template <int RB>
__global__ __launch_bounds__(256, 4) void gemm_qkv_kernel(const u16* __restrict__ A, const u16* __restrict__ Wp,
                                                          const float* __restrict__ csum, u16* __restrict__ C) {
  constexpr int RT = RB * 16;
  __shared__ __attribute__((aligned(16))) u16 As[RT * 72 * 8 > 192 * 40 ? RT * 72 * 8 : 192 * 40];
  constexpr int PF = 3, NCB = 3;
  const int tid = threadIdx.x, lane = tid & 63, cg = tid >> 6;
  const int bidx = blockIdx.x >> 3, xcd = blockIdx.x & 7;
  const int row0 = (xcd * (256 / RT) + bidx / 9) * RT;
  const int nb0 = (bidx % 9) * 12 + cg * NCB;
  const bool vblk = nb0 * 16 >= 2 * D_;
  const bf16x8* Bp = reinterpret_cast<const bf16x8*>(Wp) + (size_t)nb0 * 18 * 64 + lane;
  {
    const u32x4* src = reinterpret_cast<const u32x4*>(A + (size_t)row0 * D_);
#pragma unroll
    for (int hf = 0; hf < RB / 2; ++hf) {
      u32x4 v[9];
#pragma unroll
      for (int i = 0; i < 9; ++i) v[i] = src[(hf * 9 + i) * 256 + tid];
#pragma unroll
      for (int i = 0; i < 9; ++i) {
        const int idx = (hf * 9 + i) * 256 + tid;
        const int r = idx / 72, ch = idx - r * 72;
        *reinterpret_cast<u32x4*>(As + (r * 72 + (ch ^ ((r >> 1) & 7))) * 8) = v[i];
      }
    }
  }
  __syncthreads();
  {
    const int r = tid >> 2, p4 = tid & 3, rsw = (r >> 1) & 7;
    u32x4 v[18];
#pragma unroll
    for (int c = 0; c < 18; ++c) v[c] = *reinterpret_cast<const u32x4*>(As + (r * 72 + ((c * 4 + p4) ^ rsw)) * 8);
    float s4[4] = {0.f, 0.f, 0.f, 0.f};
#pragma unroll
    for (int c = 0; c < 18; ++c)
#pragma unroll
      for (int e = 0; e < 4; ++e) s4[e] += lo16(v[c][e]) + hi16(v[c][e]);
    float ssum = (s4[0] + s4[1]) + (s4[2] + s4[3]);
    ssum += __shfl_xor(ssum, 1);
    ssum += __shfl_xor(ssum, 2);
    const float mean = ssum * (1.0f / D_);
    float q4[4] = {0.f, 0.f, 0.f, 0.f};
#pragma unroll
    for (int c = 0; c < 18; ++c)
#pragma unroll
      for (int e = 0; e < 4; ++e) {
        const float d0 = lo16(v[c][e]) - mean, d1 = hi16(v[c][e]) - mean;
        q4[e] = fmaf(d0, d0, fmaf(d1, d1, q4[e]));
      }
    float ssq = (q4[0] + q4[1]) + (q4[2] + q4[3]);
    ssq += __shfl_xor(ssq, 1);
    ssq += __shfl_xor(ssq, 2);
    const float mb = rbf(mean);
    const float var = rbf(ssq * (1.0f / D_));
    const float rs = 1.0f / rbf(sqrtf(rbf(var + 1e-5f)));
#pragma unroll
    for (int c = 0; c < 18; ++c) {
#pragma unroll
      for (int e = 0; e < 4; ++e) {
        const unsigned dd = pack2(lo16(v[c][e]) - mb, hi16(v[c][e]) - mb);
        v[c][e] = pack2(lo16(dd) * rs, hi16(dd) * rs);
      }
      *reinterpret_cast<u32x4*>(As + (r * 72 + ((c * 4 + p4) ^ rsw)) * 8) = v[c];
    }
  }
  bf16x8 bq[PF][NCB];
#pragma unroll
  for (int s2 = 0; s2 < PF; ++s2)
#pragma unroll
    for (int j = 0; j < NCB; ++j) bq[s2][j] = Bp[(j * 18 + s2) * 64];
  __syncthreads();
  const int aoff = (lane & 15) * 72 * 8, asw = ((lane & 15) >> 1) & 7;
  f32x4 acc[RB][NCB];
#pragma unroll
  for (int i = 0; i < RB; ++i)
#pragma unroll
    for (int j = 0; j < NCB; ++j) acc[i][j] = f32x4{0.f, 0.f, 0.f, 0.f};
  auto kloop = [&](auto swapped) {
#pragma unroll
    for (int ks = 0; ks < 18; ++ks) {
      bf16x8 b[NCB];
#pragma unroll
      for (int j = 0; j < NCB; ++j) b[j] = bq[ks % PF][j];
      if (ks + PF < 18) {
#pragma unroll
        for (int j = 0; j < NCB; ++j) bq[ks % PF][j] = Bp[(j * 18 + ks + PF) * 64];
      }
#pragma unroll
      for (int i = 0; i < RB; ++i) {
        const bf16x8 a =
            *reinterpret_cast<const bf16x8*>(As + aoff + (i * 16 * 72 + ((ks * 4 + (lane >> 4)) ^ asw)) * 8);
#pragma unroll
        for (int j = 0; j < NCB; ++j) {
          if (decltype(swapped)::value)
            acc[i][j] = __builtin_amdgcn_mfma_f32_16x16x32_bf16(b[j], a, acc[i][j], 0, 0, 0);
          else
            acc[i][j] = __builtin_amdgcn_mfma_f32_16x16x32_bf16(a, b[j], acc[i][j], 0, 0, 0);
        }
      }
    }
  };
  if (!vblk) kloop(std::integral_constant<bool, true>{});
  else kloop(std::integral_constant<bool, false>{});
  __syncthreads();
  u16* ot = As;
  constexpr int QSTR = 196, VS = RT + 8;
#pragma unroll
  for (int i = 0; i < RB; ++i)
#pragma unroll
    for (int j = 0; j < NCB; ++j) {
      f32x4 v = acc[i][j];
      if (!vblk) {
        const int tl = i * 16 + (lane & 15);
        const int nl = (cg * NCB + j) * 16 + (lane >> 4) * 4;
        u32x2 w = {pack2(v[0], v[1]), pack2(v[2], v[3])};
        *reinterpret_cast<u32x2*>(ot + tl * QSTR + nl) = w;
      } else {
        const int nl = (cg * NCB + j) * 16 + (lane & 15);
        const int g = lane >> 4;
        u32x2 w = {pack2(v[0], v[1]), pack2(v[2], v[3])};
        *reinterpret_cast<u32x2*>(ot + nl * VS + i * 16 + (g & 1) * 8 + (g >> 1) * 4) = w;
      }
    }
  __syncthreads();
  const int ncol0 = (nb0 - cg * NCB) * 16;
  if (!vblk) {
    if (tid < 192) {
      const int j = tid % 96, rg = tid / 96;
      const int n = ncol0 + 2 * j;
      const int which = n >= D_;
      const int c0 = n - which * D_;
      const int h0 = c0 / DH, d0 = c0 - h0 * DH;
      u16* dst = C + (size_t)which * (H_ * S_ * 32) + ((size_t)h0 * S_ + row0) * 32 + d0;
#pragma unroll
      for (int it = 0; it < RT / 2; ++it) {
        const int tl = it * 2 + rg;
        *reinterpret_cast<unsigned*>(dst + tl * 32) = *reinterpret_cast<const unsigned*>(ot + tl * QSTR + 2 * j);
      }
    }
  } else {
#pragma unroll
    for (int it = 0; it < 6 * RB / 4; ++it) {
      const int idx = it * 256 + tid;
      const int nl = idx / (RT / 8), c8 = idx % (RT / 8);
      const int c = ncol0 + nl - 2 * D_;
      const int h = c / DH, d = c - h * DH;
      u16* dst = C + (size_t)2 * (H_ * S_ * 32) + ((size_t)h * 32 + d) * S_ + row0 + c8 * 8;
      *reinterpret_cast<u32x4*>(dst) = *reinterpret_cast<const u32x4*>(ot + nl * VS + c8 * 8);
    }
  }
}

__device__ __forceinline__ void hedgehog(const unsigned* w, const float* __restrict__ wphi, float* out) {
  float p[20];
#pragma unroll
  for (int f = 0; f < 20; ++f) p[f] = 0.f;
#pragma unroll
  for (int d = 0; d < DH; ++d) {
    const float x = (d & 1) ? hi16(w[d >> 1]) : lo16(w[d >> 1]);
#pragma unroll
    for (int f4 = 0; f4 < 5; ++f4) {
      const f32x4 ww = *reinterpret_cast<const f32x4*>(wphi + d * 20 + f4 * 4);
#pragma unroll
      for (int e = 0; e < 4; ++e) p[f4 * 4 + e] = fmaf(x, ww[e], p[f4 * 4 + e]);
    }
  }
  float mx = -INFINITY, mn = INFINITY;
#pragma unroll
  for (int f = 0; f < DH; ++f) {
    p[f] = rbf(p[f]);
    mx = fmaxf(mx, p[f]);
    mn = fminf(mn, p[f]);
  }
  const float L2E = 1.4426950408889634f;
  float s1 = 0.f, s2 = 0.f;
#pragma unroll
  for (int f = 0; f < DH; ++f) {
    out[f] = __builtin_amdgcn_exp2f((p[f] - mx) * L2E);
    out[DH + f] = __builtin_amdgcn_exp2f((mn - p[f]) * L2E);
    s1 += out[f];
    s2 += out[DH + f];
  }
  const float r1 = 1.f / s1, r2 = 1.f / s2;
#pragma unroll
  for (int f = 0; f < DH; ++f) {
    out[f] = rbf(out[f] * r1);
    out[DH + f] = rbf(out[DH + f] * r2);
  }
}

constexpr int NCH = 16;
constexpr int CT = S_ / NCH;
constexpr int PSTR = CT + 8;
__global__ __launch_bounds__(128) void kv_kernel(const u16* __restrict__ Kh, const u16* __restrict__ Vg,
                                                 const u16* __restrict__ wphi_g, float* __restrict__ part) {
  __shared__ __attribute__((aligned(16))) float wphi[DH * 20];
  __shared__ __attribute__((aligned(16))) u16 phiT[48 * PSTR];
  const int tid = threadIdx.x, lane = tid & 63, wid = tid >> 6;
  const int h = (blockIdx.x & 7) * 4 + (blockIdx.x >> 7), ch = (blockIdx.x >> 3) & 15;
  const int t0 = ch * CT;
  const u32x4* kp = reinterpret_cast<const u32x4*>(Kh + ((size_t)h * S_ + t0 + tid) * 32);
  const u32x4 k0 = kp[0], k1 = kp[1], k2 = kp[2];
  for (int i = tid; i < DH * 20; i += 128) {
    const int d = i / 20, f = i - d * 20;
    wphi[i] = f < DH ? bf2f(wphi_g[(h * DH + d) * DH + f]) : 0.f;
  }
  for (int i = tid; i < 12 * PSTR / 2; i += 128) reinterpret_cast<unsigned*>(phiT + 36 * PSTR)[i] = 0u;
  bf16x8 vb[4][2];
  {
    const int n = lane & 15, g = lane >> 4;
#pragma unroll
    for (int ks = 0; ks < 4; ++ks) {
      vb[ks][0] = *reinterpret_cast<const bf16x8*>(Vg + ((size_t)h * 32 + n) * S_ + t0 + ks * 32 + g * 8);
      const bf16x8 x = *reinterpret_cast<const bf16x8*>(Vg + ((size_t)h * 32 + 16 + (n & 1)) * S_ + t0 + ks * 32 + g * 8);
      const short c = n == 2 ? (short)0x3f80 : (short)0;
#pragma unroll
      for (int j = 0; j < 8; ++j) vb[ks][1][j] = n < 2 ? x[j] : c;
    }
  }
  __syncthreads();
  {
    const unsigned w[9] = {k0[0], k0[1], k0[2], k0[3], k1[0], k1[1], k1[2], k1[3], k2[0]};
    float ph[36];
    hedgehog(w, wphi, ph);
    const int k16 = tid & 15;
    const int pos = (tid & ~15) + ((k16 >> 2) & 1) * 8 + (k16 & 3) + 4 * (k16 >> 3);
#pragma unroll
    for (int f = 0; f < 36; ++f) phiT[f * PSTR + pos] = f2bf(ph[f]);
  }
  __syncthreads();
#pragma unroll
  for (int fb = 0; fb < 3; ++fb) {
    f32x4 acc = {0.f, 0.f, 0.f, 0.f};
#pragma unroll
    for (int ks = 0; ks < 4; ++ks) {
      const bf16x8 a = *reinterpret_cast<const bf16x8*>(phiT + (fb * 16 + (lane & 15)) * PSTR + ks * 32 + (lane >> 4) * 8);
      acc = __builtin_amdgcn_mfma_f32_16x16x32_bf16(a, wid == 0 ? vb[ks][0] : vb[ks][1], acc, 0, 0, 0);
    }
    float* dst = part + ((size_t)(h * NCH + ch) * 48 + fb * 16 + (lane >> 4) * 4) * 32 + wid * 16 + (lane & 15);
#pragma unroll
    for (int r = 0; r < 4; ++r) dst[r * 32] = acc[r];
  }
}

__global__ __launch_bounds__(128) void lin_o_kernel(const u16* __restrict__ Qh, const u16* __restrict__ wphi_g,
                                                    const float* __restrict__ part, u16* __restrict__ o) {
  __shared__ __attribute__((aligned(16))) float wphi[DH * 20];
  __shared__ __attribute__((aligned(16))) float kv[36 * 20];
  const int tid = threadIdx.x;
  const int h = (blockIdx.x & 7) * 4 + (blockIdx.x >> 7), ch = (blockIdx.x >> 3) & 15;
  const int t = ch * CT + tid;
  const u32x4* qp = reinterpret_cast<const u32x4*>(Qh + ((size_t)h * S_ + t) * 32);
  const u32x4 q0 = qp[0], q1 = qp[1], q2 = qp[2];
  for (int i = tid; i < DH * 20; i += 128) {
    const int d = i / 20, f = i - d * 20;
    wphi[i] = f < DH ? bf2f(wphi_g[(h * DH + d) * DH + f]) : 0.f;
  }
  for (int i = tid; i < 36 * 5; i += 128) {
    const int f = i / 5, d4 = i - f * 5;
    f32x4 s = {0.f, 0.f, 0.f, 0.f};
#pragma unroll
    for (int c = 0; c < NCH; ++c)
      s += *reinterpret_cast<const f32x4*>(part + ((size_t)(h * NCH + c) * 48 + f) * 32 + d4 * 4);
#pragma unroll
    for (int e = 0; e < 4; ++e) kv[f * 20 + d4 * 4 + e] = rbf(s[e]);
  }
  __syncthreads();
  const unsigned w[9] = {q0[0], q0[1], q0[2], q0[3], q1[0], q1[1], q1[2], q1[3], q2[0]};
  float ph[36];
  hedgehog(w, wphi, ph);
  float n[20];
#pragma unroll
  for (int d = 0; d < 20; ++d) n[d] = 0.f;
#pragma unroll
  for (int f = 0; f < 36; ++f)
#pragma unroll
    for (int d4 = 0; d4 < 5; ++d4) {
      const f32x4 kk = *reinterpret_cast<const f32x4*>(kv + f * 20 + d4 * 4);
#pragma unroll
      for (int e = 0; e < 4; ++e) n[d4 * 4 + e] = fmaf(ph[f], kk[e], n[d4 * 4 + e]);
    }
  const float den = rbf(rbf(n[18]) + 1e-6f);
  const float rd = 1.0f / den;
  unsigned* op = reinterpret_cast<unsigned*>(o + (size_t)t * D_ + h * DH);
#pragma unroll
  for (int d = 0; d < DH; d += 2) op[d >> 1] = pack2(rbf(n[d]) * rd, rbf(n[d + 1]) * rd);
}

constexpr int KT = 256;
constexpr int VSTR = KT + 8;
__global__ __launch_bounds__(1024, 4) void attn_kernel(const u16* __restrict__ Qh, const u16* __restrict__ Kh,
                                                       const u16* __restrict__ Vg, u16* __restrict__ o) {
  __shared__ __attribute__((aligned(16))) u16 lds_all[2 * KT * 32 + 2 * 32 * VSTR];
  u16(*Ks)[KT * 32] = reinterpret_cast<u16(*)[KT * 32]>(lds_all);
  u16(*Vt)[32 * VSTR] = reinterpret_cast<u16(*)[32 * VSTR]>(lds_all + 2 * KT * 32);
  const int tid = threadIdx.x, lane = tid & 63, wid = tid >> 6;
  const int qw = wid & 7, kh = wid >> 3;
  const int h = (blockIdx.x & 7) * 4 + (blockIdx.x >> 6);
  const int q0 = ((blockIdx.x >> 3) & 7) * 256 + qw * 32;
  const int l31 = lane & 31, hh = lane >> 5;
  for (int i = tid; i < 2 * 14 * VSTR; i += 1024) {
    const int b = i / (14 * VSTR), k = i - b * 14 * VSTR;
    Vt[b][18 * VSTR + k] = (k < VSTR) ? (u16)0x3f80 : (u16)0;
  }
  const u32x4* Kg4 = reinterpret_cast<const u32x4*>(Kh + (size_t)h * S_ * 32);
  const u32x4* Vg4 = reinterpret_cast<const u32x4*>(Vg + (size_t)h * 32 * S_);
  const int kkey = tid >> 2, kc = tid & 3;
  const int vrow = tid >> 5, vch = tid & 31;
  u32x4 kr, vr;
  auto gload = [&](int kt) {
    if (kc < 3) kr = Kg4[(kt * KT + kkey) * 4 + kc];
    if (tid < 576) vr = Vg4[(size_t)vrow * (S_ / 8) + kt * (KT / 8) + vch];
  };
  auto swrite = [&](int b) {
    const int sw = (kkey >> 2) & 3;
    u32x4 x = kr;
    if (kc == 2) { x[1] = 0u; x[2] = 0u; x[3] = 0u; }
    if (kc == 3) { x[0] = 0u; x[1] = 0u; x[2] = 0u; x[3] = 0u; }
    *reinterpret_cast<u32x4*>(&Ks[b][kkey * 32 + ((kc ^ sw) * 8)]) = x;
    if (tid < 576) *reinterpret_cast<u32x4*>(&Vt[b][vrow * VSTR + vch * 8]) = vr;
  };
  gload(0);
  const float qs = 0.23570226039551584f * 1.4426950408889634f;
  bf16x8 qf[2];
  {
    const u32x4* qp = reinterpret_cast<const u32x4*>(Qh + ((size_t)h * S_ + q0 + l31) * 32);
    const u32x4 a = qp[hh], b = qp[2];
    u32x4 w0, w1;
#pragma unroll
    for (int j = 0; j < 4; ++j) w0[j] = pack2(lo16(a[j]) * qs, hi16(a[j]) * qs);
    w1[0] = hh == 0 ? pack2(lo16(b[0]) * qs, hi16(b[0]) * qs) : 0u;
    w1[1] = 0u; w1[2] = 0u; w1[3] = 0u;
    qf[0] = __builtin_bit_cast(bf16x8, w0);
    qf[1] = __builtin_bit_cast(bf16x8, w1);
  }
  swrite(0);
  f32x16 oacc, cm;
#pragma unroll
  for (int r = 0; r < 16; ++r) { oacc[r] = 0.f; cm[r] = 0.f; }
  const int koff0 = (kh * 32 + l31) * 32 + (((0 + hh) ^ ((l31 >> 2) & 3)) * 8);
  const int koff1 = (kh * 32 + l31) * 32 + (((2 + hh) ^ ((l31 >> 2) & 3)) * 8);
  const int voff = l31 * VSTR + hh * 8 + kh * 32;
  auto qk = [&](int b, int t) {
    const bf16x8 k0 = *reinterpret_cast<const bf16x8*>(&Ks[b][koff0 + t * 64 * 32]);
    const bf16x8 k1 = *reinterpret_cast<const bf16x8*>(&Ks[b][koff1 + t * 64 * 32]);
    f32x16 s = __builtin_amdgcn_mfma_f32_32x32x16_bf16(k0, qf[0], cm, 0, 0, 0);
    return __builtin_amdgcn_mfma_f32_32x32x16_bf16(k1, qf[1], s, 0, 0, 0);
  };
  auto sm_pv = [&](f32x16& s, f32x16& sn, int b, int t) {
    int im = __float_as_int(s[0]);
#pragma unroll
    for (int r = 1; r < 15; r += 2) im = max(max(im, __float_as_int(s[r])), __float_as_int(s[r + 1]));
    im = max(im, __float_as_int(s[15]));
    {
      auto rr = __builtin_amdgcn_permlane32_swap((unsigned)im, (unsigned)im, false, false);
      im = max((int)rr[0], (int)rr[1]);
    }
    if (__any(im > 0x41000000)) {
      const float delta = __int_as_float(max(im, 0));
      const float sc = __builtin_amdgcn_exp2f(-delta);
#pragma unroll
      for (int r = 0; r < 16; ++r) {
        s[r] -= delta;
        sn[r] -= delta;
        cm[r] -= delta;
        oacc[r] *= sc;
      }
    }
    bf16x8 pf[2];
#pragma unroll
    for (int ss = 0; ss < 2; ++ss) {
      u32x4 w;
#pragma unroll
      for (int j = 0; j < 4; ++j)
        w[j] = pack2(__builtin_amdgcn_exp2f(s[8 * ss + 2 * j]), __builtin_amdgcn_exp2f(s[8 * ss + 2 * j + 1]));
      pf[ss] = __builtin_bit_cast(bf16x8, w);
    }
#pragma unroll
    for (int ss = 0; ss < 2; ++ss) {
      const bf16x8 vf = *reinterpret_cast<const bf16x8*>(&Vt[b][voff + t * 64 + ss * 16]);
      oacc = __builtin_amdgcn_mfma_f32_32x32x16_bf16(vf, pf[ss], oacc, 0, 0, 0);
    }
  };
  __syncthreads();
  f32x16 sA = qk(0, 0), sB;
  {
    float tmax = sA[0];
#pragma unroll
    for (int r = 1; r < 16; ++r) tmax = fmaxf(tmax, sA[r]);
    tmax = fmaxf(tmax, __shfl_xor(tmax, 32));
#pragma unroll
    for (int r = 0; r < 16; ++r) { sA[r] -= tmax; cm[r] = -tmax; }
  }
  for (int kt = 0; kt < S_ / KT; ++kt) {
    const int b = kt & 1;
    const bool more = kt + 1 < S_ / KT;
    if (more) gload(kt + 1);
    sB = qk(b, 1);
    sm_pv(sA, sB, b, 0);
    sA = qk(b, 2);
    sm_pv(sB, sA, b, 1);
    sB = qk(b, 3);
    sm_pv(sA, sB, b, 2);
    if (more) {
      swrite(b ^ 1);
      __syncthreads();
      sA = qk(b ^ 1, 0);
    } else {
#pragma unroll
      for (int r = 0; r < 16; ++r) sA[r] = 0.f;
    }
    sm_pv(sB, sA, b, 3);
    __syncthreads();
  }
  float* mg = reinterpret_cast<float*>(lds_all);
  if (kh == 1) {
#pragma unroll
    for (int r = 0; r < 16; ++r) mg[(qw * 17 + r) * 64 + lane] = oacc[r];
    mg[(qw * 17 + 16) * 64 + lane] = cm[0];
  }
  __syncthreads();
  if (kh == 1) return;
  const float m0 = -cm[0], m1 = -mg[(qw * 17 + 16) * 64 + lane];
  const float mm = fmaxf(m0, m1);
  const float a0 = __builtin_amdgcn_exp2f(m0 - mm), a1 = __builtin_amdgcn_exp2f(m1 - mm);
#pragma unroll
  for (int r = 0; r < 16; ++r) oacc[r] = oacc[r] * a0 + mg[(qw * 17 + r) * 64 + lane] * a1;
  const float l = __shfl(oacc[10], l31);
  const float rl = 1.f / l;
  u16* op = o + (size_t)(q0 + l31) * D_ + h * DH;
#pragma unroll
  for (int r = 0; r < 16; ++r) {
    const int d = (r & 3) + 8 * (r >> 2) + 4 * hh;
    if (d < DH) op[d] = f2bf(oacc[r] * rl);
  }
}

extern "C" void kernel_launch(void* const* d_in, const int* in_sizes, int n_in, void* d_out, int out_size,
                              void* d_ws, size_t ws_size, hipStream_t stream) {
  const u16* A_x = (const u16*)d_in[0];
  const u16* A_wqkv = (const u16*)d_in[1];
  const u16* A_wphi = (const u16*)d_in[2];
  const u16* A_wo = (const u16*)d_in[3];
  const u16* B_wqkv = (const u16*)d_in[5];
  const u16* B_wo = (const u16*)d_in[6];
  char* ws = (char*)d_ws;
  size_t off = 0;
  auto alloc = [&](size_t bytes) { void* p = ws + off; off += (bytes + 255) & ~(size_t)255; return p; };
  u16* PAq = (u16*)alloc((size_t)D_ * N3 * 2);
  u16* PBq = (u16*)alloc((size_t)D_ * N3 * 2);
  u16* PAo = (u16*)alloc((size_t)D_ * D_ * 2);
  u16* PBo = (u16*)alloc((size_t)D_ * D_ * 2);
  u16* qkv = (u16*)alloc((size_t)3 * H_ * S_ * 32 * 2);
  u16* ob = (u16*)alloc((size_t)S_ * D_ * 2);
  u16* x1 = (u16*)alloc((size_t)S_ * D_ * 2);
  u16* x2 = (u16*)alloc((size_t)S_ * D_ * 2);
  u16* x3 = (u16*)alloc((size_t)S_ * D_ * 2);
  float* part = (float*)alloc((size_t)H_ * NCH * 48 * 32 * 4);

  float* csum = (float*)alloc((size_t)2 * N3 * 4);
  pack_w_kernel<<<dim3(108 * 18 * 64 / 256, 4), 256, 0, stream>>>(A_wqkv, B_wqkv, A_wo, B_wo, PAq, PBq, PAo, PBo, csum);

  const u16* xin = A_x;
  u16* xs[4] = {x1, x2, x3, (u16*)d_out};
  for (int layer = 0; layer < 4; ++layer) {
    const bool isA = (layer & 1) == 0;
    if (isA) {
      gemm_qkv_kernel<4><<<dim3(9 * 32), 256, 0, stream>>>(xin, PAq, csum, qkv);
      kv_kernel<<<dim3(512), 128, 0, stream>>>(qkv + (size_t)H_ * S_ * 32, qkv + (size_t)2 * H_ * S_ * 32, A_wphi, part);
      lin_o_kernel<<<dim3(512), 128, 0, stream>>>(qkv, A_wphi, part, ob);
    } else {
      gemm_qkv_kernel<4><<<dim3(9 * 32), 256, 0, stream>>>(xin, PBq, csum + N3, qkv);
      attn_kernel<<<dim3(256), 1024, 0, stream>>>(qkv, qkv + (size_t)H_ * S_ * 32, qkv + (size_t)2 * H_ * S_ * 32, ob);
    }
    gemm_wo_kernel<<<dim3(9 * 32), 512, 0, stream>>>(ob, isA ? PAo : PBo, xin, xs[layer]);
    xin = xs[layer];
  }
}
```
